```python
import torch

def _softmax(x, axis=-1):
    return torch.softmax(x, dim=axis)
def _gelu(x):
    return torch.nn.functional.gelu(x, approximate="tanh")
def _silu(x):
    return torch.nn.functional.silu(x)
def _sigmoid(x):
    return torch.sigmoid(x)
def _layernorm(x, axis=-1, eps=1e-5):
    m = x.mean(dim=axis, keepdim=True)
    v = x.var(dim=axis, keepdim=True, unbiased=False)
    return (x - m) / torch.sqrt(v + eps)
def _scatter_add(tgt, idx, src, axis=0):
    return tgt.clone().index_add_(axis, idx, src)
def _segment_sum(src, seg, n):
    out = torch.zeros((n,) + src.shape[1:], dtype=src.dtype)
    return out.index_add_(0, seg, src)
def _pad(x, pw):
    flat = tuple(v for pair in reversed(pw) for v in pair)
    return torch.nn.functional.pad(x, flat)

kernel_name = 'interleave_layers__sliding_local_dilated__linear_kernel_hedgehog__prompt'

def setup_inputs():

    _w = (lambda a: (lambda t: t.to(torch.bfloat16) if t.is_floating_point() else t)(torch.as_tensor(a)))
    def _A_setup():
        B, S, D = 1, 2048, 640
        return {
            "x": _w((lambda *s: torch.randn(*s, dtype=torch.bfloat16) * 0.05)(B, S, D)),
            "wqkv": _w((lambda *s: torch.randn(*s, dtype=torch.bfloat16) * 0.05)(D, 3 * D)),
            "wo": _w((lambda *s: torch.randn(*s, dtype=torch.bfloat16) * 0.05)(D, D)),
        }
    _A_in = _A_setup()

    def _B_setup():
        B, S, D, H = 1, 2048, 640, 32
        Dh = D // H
        return {
            "x": _w((lambda *s: torch.randn(*s, dtype=torch.bfloat16) * 0.05)(B, S, D)),
            "wqkv": _w((lambda *s: torch.randn(*s, dtype=torch.bfloat16) * 0.4)(D, 3 * D)),
            "w_phi": _w((lambda *s: torch.randn(*s, dtype=torch.bfloat16) * 0.4)(H, Dh, Dh) * (Dh ** -0.5)),
            "wo": _w((lambda *s: torch.randn(*s, dtype=torch.bfloat16) * 0.1)(D, D)),
        }
    _B_in = _B_setup()

    return {
        "A_x": _A_in["x"],
        "A_wqkv": _A_in["wqkv"],
        "A_wo": _A_in["wo"],
        "B_x": _B_in["x"],
        "B_wqkv": _B_in["wqkv"],
        "B_w_phi": _B_in["w_phi"],
        "B_wo": _B_in["wo"],
    }

def reference(A_x, A_wqkv, A_wo, B_x, B_wqkv, B_w_phi, B_wo):
    def _A_ref(x, wqkv, wo):
        B, S, D = x.shape
        H = 32; Dh = D // H
        W, dil = 8, 2
        qkv = (lambda x, *d: x.permute(*d))((x @ wqkv).reshape(B, S, 3, H, Dh), 2, 0, 3, 1, 4)
        q, k, v = qkv[0], qkv[1], qkv[2]
        scale = Dh ** -0.5
        pos = torch.arange(S)
        off = torch.arange(W) * dil
        idx = pos[:, None] - off[None, :]
        mask = idx >= 0
        idx_c = torch.where(mask, idx, 0).reshape(-1)
        kg = k[:, :, idx_c, :].reshape(B, H, S, W, Dh)
        vg = v[:, :, idx_c, :].reshape(B, H, S, W, Dh)
        logits = torch.einsum("bhsd,bhswd->bhsw", q, kg) * scale
        logits = torch.where(mask[None, None, :, :], logits, -1e9)
        attn = _softmax(logits, axis=-1)
        o = torch.einsum("bhsw,bhswd->bhsd", attn, vg)
        return (lambda x, *d: x.permute(*d))(o, 0, 2, 1, 3).reshape(B, S, D) @ wo

    def _B_ref(x, wqkv, w_phi, wo):
        B, S, D = x.shape
        H = 32; Dh = D // H
        qkv = (lambda x, *d: x.permute(*d))((x @ wqkv).reshape(B, S, 3, H, Dh), 2, 0, 3, 1, 4)
        q, k, v = qkv[0], qkv[1], qkv[2]
        pq = torch.einsum("bhsd,hdf->bhsf", q, w_phi)
        pk = torch.einsum("bhsd,hdf->bhsf", k, w_phi)
        phi_q = torch.concatenate([_softmax(pq, axis=-1), _softmax(-pq, axis=-1)], axis=-1)
        phi_k = torch.concatenate([_softmax(pk, axis=-1), _softmax(-pk, axis=-1)], axis=-1)
        kv = torch.einsum("bhsf,bhsd->bhfd", phi_k, v)
        z  = torch.einsum("bhsf->bhf", phi_k)
        num = torch.einsum("bhqf,bhfd->bhqd", phi_q, kv)
        den = torch.einsum("bhqf,bhf->bhq", phi_q, z)[..., None] + 1e-6
        o = num / den
        return (lambda x, *d: x.permute(*d))(o, 0, 2, 1, 3).reshape(B, S, D) @ wo

    x = A_x
    x = x + _A_ref(_layernorm(x, axis=-1), A_wqkv, A_wo)
    x = x + _B_ref(_layernorm(x, axis=-1), B_wqkv, B_w_phi, B_wo)
    x = x + _A_ref(_layernorm(x, axis=-1), A_wqkv, A_wo)
    x = x + _B_ref(_layernorm(x, axis=-1), B_wqkv, B_w_phi, B_wo)
    return x
```

```cpp
#include <hip/hip_runtime.h>
#include <hip/hip_bf16.h>
typedef unsigned short u16;
constexpr int S=2048, D=640, H=32, DH=20;
__device__ __forceinline__ float bf2f(u16 b){ return __uint_as_float(((unsigned)b)<<16); }
__device__ __forceinline__ u16 f2bf(float f){ __hip_bfloat16 h = __float2bfloat16(f); return *(u16*)&h; }
__device__ __forceinline__ float rbf(float f){ return bf2f(f2bf(f)); }

__global__ void k_qkv(const u16* x, const u16* W, float* qkv){
  int s = blockIdx.x; __shared__ float xs[D]; __shared__ float red[2];
  for(int i=threadIdx.x;i<D;i+=blockDim.x) xs[i]=bf2f(x[s*D+i]);
  __syncthreads();
  if(threadIdx.x==0){ float m=0; for(int i=0;i<D;i++) m+=xs[i]; m/=D; float v=0; for(int i=0;i<D;i++){float d=xs[i]-m; v+=d*d;} v/=D; red[0]=m; red[1]=rsqrtf(v+1e-5f);}
  __syncthreads();
  float m=red[0], r=red[1];
  for(int n=threadIdx.x;n<3*D;n+=blockDim.x){ float a=0,c=0; for(int k=0;k<D;k++){ float w=bf2f(W[k*3*D+n]); a+=xs[k]*w; c+=w;} qkv[s*3*D+n]=r*(a-m*c);}
}
__global__ void k_attA(const float* qkv, u16* o){
  int idx=blockIdx.x*blockDim.x+threadIdx.x; if(idx>=S*H) return; int s=idx/H,h=idx%H;
  float l[8]; float mx=-1e30f; const float sc=rsqrtf(20.f);
  for(int j=0;j<8;j++){ int t=s-2*j; if(t<0){l[j]=-1e9f;} else { float a=0; for(int d=0;d<DH;d++) a+=rbf(qkv[s*3*D+h*DH+d])*rbf(qkv[t*3*D+D+h*DH+d]); l[j]=a*sc;} mx=fmaxf(mx,l[j]); }
  float sum=0; for(int j=0;j<8;j++){ l[j]=__expf(l[j]-mx); sum+=l[j]; }
  for(int d=0;d<DH;d++){ float a=0; for(int j=0;j<8;j++){ int t=s-2*j; if(t<0)t=0; a+=l[j]*rbf(qkv[t*3*D+2*D+h*DH+d]); } o[s*D+h*DH+d]=f2bf(a/sum);}
}
__global__ void k_phi(const float* qkv, const u16* wphi, float* phiq, float* phik){
  int idx=blockIdx.x*blockDim.x+threadIdx.x; if(idx>=S*H*2) return; int w=idx&1; int s=(idx>>1)/H,h=(idx>>1)%H;
  float p[DH]; float mx=-1e30f,mn=1e30f;
  for(int f=0;f<DH;f++){ float a=0; for(int d=0;d<DH;d++) a+=qkv[s*3*D+w*D+h*DH+d]*bf2f(wphi[(h*DH+d)*DH+f]); p[f]=a; mx=fmaxf(mx,a); mn=fminf(mn,a);}
  float s1=0,s2=0; float e1[DH],e2[DH]; for(int f=0;f<DH;f++){ e1[f]=__expf(p[f]-mx); e2[f]=__expf(mn-p[f]); s1+=e1[f]; s2+=e2[f]; }
  float* o=(w?phik:phiq)+(s*H+h)*40; for(int f=0;f<DH;f++){ o[f]=rbf(e1[f]/s1); o[20+f]=rbf(e2[f]/s2);}
}
__global__ void k_kv(const float* qkv, const float* phik, float* kv){
  int idx=blockIdx.x*blockDim.x+threadIdx.x; if(idx>=H*40*21) return; int h=idx/(840), f=(idx%840)/21, d=idx%21;
  float a=0; for(int s=0;s<S;s++){ float v = d<20? rbf(qkv[s*3*D+2*D+h*DH+d]) : 1.f; a+=phik[(s*H+h)*40+f]*v; } kv[idx]=a;
}
__global__ void k_oB(const float* phiq, const float* kv, u16* o){
  int idx=blockIdx.x*blockDim.x+threadIdx.x; if(idx>=S*H) return; int s=idx/H,h=idx%H;
  float den=1e-6f; for(int f=0;f<40;f++) den+=phiq[(s*H+h)*40+f]*rbf(kv[h*840+f*21+20]);
  for(int d=0;d<DH;d++){ float a=0; for(int f=0;f<40;f++) a+=phiq[(s*H+h)*40+f]*rbf(kv[h*840+f*21+d]); o[s*D+h*DH+d]=f2bf(a/den);}
}
__global__ void k_op(const u16* o, const u16* Wo, const u16* x, u16* xn){
  int s=blockIdx.x; __shared__ float os[D]; for(int i=threadIdx.x;i<D;i+=blockDim.x) os[i]=bf2f(o[s*D+i]); __syncthreads();
  for(int n=threadIdx.x;n<D;n+=blockDim.x){ float a=0; for(int k=0;k<D;k++) a+=os[k]*bf2f(Wo[k*D+n]); xn[s*D+n]=f2bf(bf2f(x[s*D+n])+a);}
}
extern "C" void kernel_launch(void* const* d_in, const int* in_sizes, int n_in, void* d_out, int out_size, void* d_ws, size_t ws_size, hipStream_t stream){
  const u16* x=(const u16*)d_in[0]; const u16* Aq=(const u16*)d_in[1]; const u16* Ao=(const u16*)d_in[2];
  const u16* Bq=(const u16*)d_in[4]; const u16* Bp=(const u16*)d_in[5]; const u16* Bo=(const u16*)d_in[6];
  char* w=(char*)d_ws; float* qkv=(float*)w; w+=S*3*D*4; float* phiq=(float*)w; w+=S*H*40*4; float* phik=(float*)w; w+=S*H*40*4; float* kv=(float*)w; w+=H*840*4; u16* o=(u16*)w; w+=S*D*2; u16* x1=(u16*)w; w+=S*D*2; u16* x2=(u16*)w;
  const u16* cur=x; u16* nx[4]={x1,x2,x1,(u16*)d_out};
  for(int L=0;L<4;L++){
    if((L&1)==0){ k_qkv<<<S,256,0,stream>>>(cur,Aq,qkv); k_attA<<<S*H/256,256,0,stream>>>(qkv,o); k_op<<<S,256,0,stream>>>(o,Ao,cur,nx[L]); }
    else { k_qkv<<<S,256,0,stream>>>(cur,Bq,qkv); k_phi<<<S*H*2/256,256,0,stream>>>(qkv,Bp,phiq,phik); k_kv<<<(H*840+255)/256,256,0,stream>>>(qkv,phik,kv); k_oB<<<S*H/256,256,0,stream>>>(phiq,kv,o); k_op<<<S,256,0,stream>>>(o,Bo,cur,nx[L]); }
    cur=nx[L];
  }
}
```

```cpp
#include <hip/hip_runtime.h>
#include <hip/hip_bf16.h>

typedef unsigned short u16;
typedef unsigned u32;
typedef unsigned long long u64;
typedef u32 v4u __attribute__((ext_vector_type(4)));
typedef u32 v2u __attribute__((ext_vector_type(2)));
typedef int v4i __attribute__((ext_vector_type(4)));
typedef float v4f __attribute__((ext_vector_type(4)));
typedef float v2f __attribute__((ext_vector_type(2)));
typedef float v16f __attribute__((ext_vector_type(16)));
typedef __bf16 v8bf __attribute__((ext_vector_type(8)));
typedef __bf16 v4bf __attribute__((ext_vector_type(4)));
typedef __bf16 v2bf __attribute__((ext_vector_type(2)));
#define LAS __attribute__((address_space(3)))
#define GAS __attribute__((address_space(1)))
typedef LAS char* lptr;

#define PROF 0

constexpr u32 WS_FLAG = 4096;
constexpr u32 WS_CS = WS_FLAG + 16 * 256 * 128;
constexpr u32 WS_ST = WS_CS + 2 * 16 * 16 * 1024;
constexpr u32 WS_HK = WS_ST + 4 * 16 * 4 * 8 * 512;
constexpr u32 WS_KV = WS_HK + 2 * 256 * 4096;
constexpr u32 WS_WQ = WS_KV + 2 * 256 * 3584;
constexpr u32 WS_WO = WS_WQ + 2 * 16 * 153600;
constexpr u32 WS_X = WS_WO + 2 * 8 * 102400;
constexpr u32 WS_O = WS_X + 4 * 64 * 40960;
constexpr u32 WS_DBG = WS_O + 4 * 128 * 20480;
constexpr u32 WS_END = WS_DBG + 256 * 4 * 64 * 8;
constexpr int R1_BLK = 704, R1_STEP = 2816, R2_OFF = 112640, WIMG = 153600;
constexpr int L_CS = WIMG;
constexpr int L_KVR = L_CS + 1024;
constexpr int L_WPHI = L_KVR + 3520;
constexpr int L_WO = 36864;
constexpr int L_VP = 0, L_KP = 23040;
constexpr int L_PA = 0, L_PB = 16384;
constexpr int LDS_BYTES = 163840;
constexpr int AUX_SC1 = 16;
constexpr u32 SPIN_CAP = 1u << 16;
enum { PH_F0 = 0, PH_F1 = 1, PH_O = 2, PH_X = 6, PH_M = 10 };

struct Params {
  const u16* x0; const u16* wqA; const u16* woA; const u16* wqB; const u16* wphi; const u16* woB;
  u16* out; unsigned char* ws;
};

typedef __amdgpu_buffer_rsrc_t rsrc_t;
__device__ __forceinline__ rsrc_t mk_rsrc(const void* p, u32 bytes) { return __builtin_amdgcn_make_buffer_rsrc((void*)p, 0, bytes, 0x00020000); }
__device__ __forceinline__ v4u ld16(rsrc_t r, u32 vo, u32 so) { return __builtin_amdgcn_raw_buffer_load_b128(r, vo, so, AUX_SC1); }
__device__ __forceinline__ v2u ld8(rsrc_t r, u32 vo, u32 so) { return __builtin_amdgcn_raw_buffer_load_b64(r, vo, so, AUX_SC1); }
__device__ __forceinline__ u32 ld4(rsrc_t r, u32 vo, u32 so) { return __builtin_amdgcn_raw_buffer_load_b32(r, vo, so, AUX_SC1); }
__device__ __forceinline__ v4u ld16p(rsrc_t r, u32 vo, u32 so) { return __builtin_amdgcn_raw_buffer_load_b128(r, vo, so, 0); }
__device__ __forceinline__ v2u ld8p(rsrc_t r, u32 vo, u32 so) { return __builtin_amdgcn_raw_buffer_load_b64(r, vo, so, 0); }
__device__ __forceinline__ void st16(v4u v, rsrc_t r, u32 vo, u32 so) { __builtin_amdgcn_raw_buffer_store_b128(v, r, vo, so, AUX_SC1); }
__device__ __forceinline__ void st8(v2u v, rsrc_t r, u32 vo, u32 so) { __builtin_amdgcn_raw_buffer_store_b64(v, r, vo, so, AUX_SC1); }
__device__ __forceinline__ void st4(u32 v, rsrc_t r, u32 vo, u32 so) { __builtin_amdgcn_raw_buffer_store_b32(v, r, vo, so, AUX_SC1); }
#define DRAIN() asm volatile("s_waitcnt vmcnt(0)" ::: "memory")
#define VMWAIT(n) asm volatile("s_waitcnt vmcnt(" #n ")" ::: "memory")
#define SB() __builtin_amdgcn_sched_barrier(0)
__device__ __forceinline__ void wg_barrier() {
  asm volatile("s_waitcnt lgkmcnt(0)\n\ts_barrier" ::: "memory");
}
__device__ __forceinline__ void dma(v4i rs, u32 voff, u32 soff, u32 lds) {
  u32 keep;
  asm volatile("s_mov_b32 %0, m0\n\ts_mov_b32 m0, %4\n\ts_nop 0\n\tbuffer_load_dwordx4 %1, %2, %3 offen sc1 lds\n\ts_mov_b32 m0, %0"
               : "=&s"(keep) : "v"(voff), "s"(rs), "s"(soff), "s"(lds) : "memory");
}
__device__ __forceinline__ u32 pk2(float a, float b) { v2f f = {a, b}; return __builtin_bit_cast(u32, __builtin_convertvector(f, v2bf)); }
__device__ __forceinline__ float bflo(u32 d) { return __uint_as_float(d << 16); }
__device__ __forceinline__ float bfhi(u32 d) { return __uint_as_float(d & 0xffff0000u); }
__device__ __forceinline__ v8bf asbf(v4u v) { return __builtin_bit_cast(v8bf, v); }
__device__ __forceinline__ v8bf trfrag(lptr p0, lptr p1) {
  v4bf a = __builtin_amdgcn_ds_read_tr16_b64_v4bf16((LAS v4bf*)p0);
  v4bf b = __builtin_amdgcn_ds_read_tr16_b64_v4bf16((LAS v4bf*)p1);
  return __builtin_shufflevector(a, b, 0, 1, 2, 3, 4, 5, 6, 7);
}
__device__ __forceinline__ v16f mfma32(v8bf a, v8bf b, v16f c) { return __builtin_amdgcn_mfma_f32_32x32x16_bf16(a, b, c, 0, 0, 0); }
__device__ __forceinline__ v4f mfma16(v8bf a, v8bf b, v4f c) { return __builtin_amdgcn_mfma_f32_16x16x32_bf16(a, b, c, 0, 0, 0); }
__device__ __forceinline__ float dot2(u32 a, u32 b, float c) {
  return __builtin_amdgcn_fdot2_f32_bf16(__builtin_bit_cast(v2bf, a), __builtin_bit_cast(v2bf, b), c, false);
}
__device__ __forceinline__ void poll_flags(rsrc_t ws, u32 voff, bool on, u32 tag) {
  for (u32 sp = 0; sp < SPIN_CAP; ++sp) {
    u32 v = tag;
    asm volatile("" ::: "memory");
    if (on) v = ld4(ws, voff, 0);
    if (__all(v == tag)) break;
  }
  __builtin_amdgcn_fence(__ATOMIC_ACQUIRE, "wavefront");
}
__device__ __forceinline__ u32 flag_off(int ph, int cu) { return WS_FLAG + (u32)(ph * 256 + cu) * 128u; }

#define STAMP(i) do {} while (0)

__device__ __forceinline__ void dual_softmax(const float (&pv)[20], u32 (&phi)[20]) {
  const float C = 1.4426950408889634f;
  float mx = pv[0], mn = pv[0];
#pragma unroll
  for (int i = 1; i < 20; ++i) { mx = fmaxf(mx, pv[i]); mn = fminf(mn, pv[i]); }
  const float a = mx * C, b = mn * C;
  float ep[20], en[20], sp = 0.f, sn = 0.f;
#pragma unroll
  for (int i = 0; i < 20; ++i) {
    ep[i] = __builtin_amdgcn_exp2f(__builtin_fmaf(pv[i], C, -a));
    en[i] = __builtin_amdgcn_exp2f(__builtin_fmaf(pv[i], -C, b));
    sp += ep[i]; sn += en[i];
  }
  const float rp = __builtin_amdgcn_rcpf(sp), rn = __builtin_amdgcn_rcpf(sn);
#pragma unroll
  for (int i = 0; i < 10; ++i) { phi[i] = pk2(ep[2 * i] * rp, ep[2 * i + 1] * rp); phi[10 + i] = pk2(en[2 * i] * rn, en[2 * i + 1] * rn); }
}
__device__ __forceinline__ void feat_proj(const u32 (&t)[10], const v4u (&wf)[6], float (&pv)[20]) {
  const v4u b0 = {t[0], t[1], t[2], t[3]}, b1 = {t[4], t[5], t[6], t[7]}, b2 = {t[8], t[9], 0u, 0u};
  v16f X = {}, Y = {};
  X = mfma32(asbf(wf[0]), asbf(b0), X); Y = mfma32(asbf(wf[3]), asbf(b0), Y);
  X = mfma32(asbf(wf[1]), asbf(b1), X); Y = mfma32(asbf(wf[4]), asbf(b1), Y);
  X = mfma32(asbf(wf[2]), asbf(b2), X); Y = mfma32(asbf(wf[5]), asbf(b2), Y);
#pragma unroll
  for (int i = 0; i < 16; ++i) pv[i] = X[i];
#pragma unroll
  for (int i = 0; i < 4; ++i) pv[16 + i] = Y[i];
}

__global__ void __launch_bounds__(256, 1) fused_kernel(Params p) {
  extern __shared__ __attribute__((aligned(16))) char smem[];
  const lptr L0 = (lptr)smem;
  const int tid = threadIdx.x, lane = tid & 63, w = __builtin_amdgcn_readfirstlane(tid >> 6);
  const int b = blockIdx.x;
  const int tg = (b & 7) * 2 + (b >> 7), j = (b >> 3) & 15;
  const int cu = tg * 16 + j, th = j & 1, cg = j >> 1;
  const rsrc_t ws = mk_rsrc(p.ws, WS_END);
  v4i wsd; { const u64 a = (u64)p.ws; wsd.x = (int)(u32)a; wsd.y = (int)(u32)(a >> 32); wsd.z = (int)WS_END; wsd.w = 0x00020000; }
  const int tok = lane & 31, h = lane >> 5;
  const int t16 = lane & 15, g = lane >> 4;
  STAMP(0);

  const u32 epoch_v = ld4(ws, 0, 0);
  float res[20];
  v4u wf[6];
  u32 E;
  {
    const rsrc_t rx = mk_rsrc(p.x0, 2048 * 640 * 2), rA = mk_rsrc(p.wqA, 640 * 1920 * 2), rB = mk_rsrc(p.wqB, 640 * 1920 * 2);
    const rsrc_t roA = mk_rsrc(p.woA, 640 * 640 * 2), roB = mk_rsrc(p.woB, 640 * 640 * 2), rp = mk_rsrc(p.wphi, 32 * 400 * 2);
    u32 src[3], dst[3], stg[3];
#pragma unroll
    for (int i = 0; i < 3; ++i) {
      const int n = tid + 256 * i;
      int row, part, mem;
      if (n < 440) {
        const int kb = n / 44, cc = n % 44;
        if (cc < 32) { row = kb * 4 + ((cc >> 2) & 3); part = 1 + (cc >> 4); mem = (cc & 3) + ((cc >> 1) & 1); }
        else { const int t = cc - 32, m = t % 3; row = kb * 4 + t / 3; part = (m + 1) % 3; mem = 2; }
        dst[i] = (u32)(10 * tg * R1_BLK + n * 16);
      } else {
        const int n2 = n - 440;
        row = n2 >> 2; part = 0; mem = (n2 & 3) + ((n2 >> 1) & 1);
        dst[i] = (u32)(R2_OFF + 10 * tg * 256 + n2 * 16);
      }
      src[i] = (u32)((40 * tg + row) * 3840 + part * 1280 + j * 80 + mem * 16);
      stg[i] = (u32)(row * 240 + part * 80 + mem * 16);
    }
    const bool on2 = tid < 88;
    v4u ca[3], cb[3];
    ca[0] = ld16p(rA, src[0], 0); ca[1] = ld16p(rA, src[1], 0);
    if (on2) ca[2] = ld16p(rA, src[2], 0);
    v2u xr[5];
    const u32 xo = (u32)((128 * tg + 64 * th + 16 * w + t16) * 1280 + (80 * cg + 4 * g) * 2);
#pragma unroll
    for (int c = 0; c < 5; ++c) xr[c] = ld8p(rx, xo + 32 * c, 0);
    cb[0] = ld16p(rB, src[0], 0); cb[1] = ld16p(rB, src[1], 0);
    if (on2) cb[2] = ld16p(rB, src[2], 0);
    const int wi = 2 * tg + th;
    v4u co[4];
    u32 osrc[2];
#pragma unroll
    for (int i = 0; i < 2; ++i) {
      const int n = tid + 256 * i, bc = n >> 4, blk = bc / 5, c = bc % 5, i8 = (n >> 1) & 7;
      const int k = 32 * wi + 8 * (2 * (blk & 1) + (i8 >> 2)) + 4 * (blk >> 1) + (i8 & 3);
      osrc[i] = (u32)(k * 1280 + (80 * cg + 16 * c) * 2 + (n & 1) * 16);
    }
    if (wi < 20) {
      co[0] = ld16p(roA, osrc[0], 0); co[2] = ld16p(roB, osrc[0], 0);
      if (tid < 64) { co[1] = ld16p(roA, osrc[1], 0); co[3] = ld16p(roB, osrc[1], 0); }
    }
    v4u cp;
    if (tid < 100) cp = ld16p(rp, (u32)(j * 1600 + tid * 16), 0);
    SB();
    E = __builtin_amdgcn_readfirstlane(epoch_v);
    const u32 qa = WS_WQ + (u32)j * WIMG;
    st16(ca[0], ws, dst[0], qa); st16(ca[1], ws, dst[1], qa);
    if (on2) st16(ca[2], ws, dst[2], qa);
    {
      const u32 xb = WS_X + (u32)(tg * 4 + th * 2 + (w >> 1)) * 40960u + (u32)(5 * cg) * 1024u;
      const u32 tk = (u32)((w & 1) * 16 + t16);
#pragma unroll
      for (int c = 0; c < 4; c += 2) {
        auto r0 = __builtin_amdgcn_permlane32_swap(xr[c].x, xr[c + 1].x, false, false);
        auto r1 = __builtin_amdgcn_permlane32_swap(xr[c].y, xr[c + 1].y, false, false);
        st16((v4u){r0[0], r1[0], r0[1], r1[1]}, ws, (u32)(c + h) * 1024u + ((u32)(g & 1) * 32u + tk) * 16u, xb);
      }
      st8(xr[4], ws, 4096u + ((u32)(g & 1) * 32u + tk) * 16u + (u32)(g >> 1) * 8u, xb);
      float s1 = 0.f, s2 = 0.f;
#pragma unroll
      for (int c = 0; c < 5; ++c) {
        res[4 * c] = bflo(xr[c].x); res[4 * c + 1] = bfhi(xr[c].x); res[4 * c + 2] = bflo(xr[c].y); res[4 * c + 3] = bfhi(xr[c].y);
      }
#pragma unroll
      for (int i = 0; i < 20; ++i) { s1 += res[i]; s2 = __builtin_fmaf(res[i], res[i], s2); }
      s1 += __shfl_xor(s1, 16); s2 += __shfl_xor(s2, 16);
      s1 += __shfl_xor(s1, 32); s2 += __shfl_xor(s2, 32);
      if (lane < 16)
        st16((v4u){__float_as_uint(s1), E + 1u, __float_as_uint(s2), E + 1u}, ws, tk * 16u,
             WS_ST + (u32)((tg * 4 + th * 2 + (w >> 1)) * 8 + cg) * 512u);
    }
    *(LAS v4u*)(L0 + stg[0]) = ca[0]; *(LAS v4u*)(L0 + stg[1]) = ca[1];
    *(LAS v4u*)(L0 + 9600 + stg[0]) = cb[0]; *(LAS v4u*)(L0 + 9600 + stg[1]) = cb[1];
    if (on2) { *(LAS v4u*)(L0 + stg[2]) = ca[2]; *(LAS v4u*)(L0 + 9600 + stg[2]) = cb[2]; }
    if (tid < 100) *(LAS v4u*)(L0 + L_WPHI + tid * 16) = cp;
    if (tid < 10) *(LAS v4u*)(L0 + L_KVR + tid * 352 + 336) = (v4u){0u, 0u, 0u, 0u};
    DRAIN();
    wg_barrier();
    if (tid == 0) st4(E + 1u, ws, flag_off(PH_F0, cu), 0);
    STAMP(1);
    const u32 qb = WS_WQ + (u32)(16 + j) * WIMG;
    st16(cb[0], ws, dst[0], qb); st16(cb[1], ws, dst[1], qb);
    if (on2) st16(cb[2], ws, dst[2], qb);
    if (wi < 20) {
      const u32 oa = WS_WO + (u32)cg * 102400u + (u32)wi * 5120u, ob = oa + 8u * 102400u;
      st16(co[0], ws, tid * 16u, oa); st16(co[2], ws, tid * 16u, ob);
      if (tid < 64) { st16(co[1], ws, 4096u + tid * 16u, oa); st16(co[3], ws, 4096u + tid * 16u, ob); }
    }
    if (tid < 240) {
      const int ty = tid / 120, col = tid % 120, part = col / 40, hh = (col % 40) / 20, d = col % 20;
      float s = 0.f;
#pragma unroll 8
      for (int r = 0; r < 40; ++r) s += __uint_as_float((u32) * (LAS u16*)(L0 + ty * 9600 + r * 240 + col * 2) << 16);
      const int T = (part + 2) % 3, e = hh ? (d + 16) % 20 : d;
      const int idx = hh * 64 + (e < 16 ? T * 16 + e : 48 + 4 * T + e - 16);
      st8((v2u){__float_as_uint(s), E + 1u}, ws, (u32)idx * 8u, WS_CS + (u32)((ty * 16 + j) * 16 + tg) * 1024u);
    }
    {
      const int r = lane & 31, m = r >> 3, hp = (r >> 2) & 1, t = r & 3;
#pragma unroll
      for (int y = 0; y < 2; ++y)
#pragma unroll
        for (int i = 0; i < 3; ++i) {
          const int f = y ? 16 + t : 4 * m + t;
          const bool ok = (hp == h) && (y == 0 || m == 0);
          u32 dw[4];
#pragma unroll
          for (int q = 0; q < 4; ++q) {
            u32 lo = 0, hi = 0;
            const int e0 = 8 * i + 2 * q;
            if (e0 < 20) {
              const int d0 = h ? (e0 + 4) % 20 : e0, d1 = h ? (e0 + 5) % 20 : e0 + 1;
              lo = *(LAS u16*)(L0 + L_WPHI + (h * 400 + d0 * 20 + f) * 2);
              hi = *(LAS u16*)(L0 + L_WPHI + (h * 400 + d1 * 20 + f) * 2);
            }
            dw[q] = ok ? (lo | (hi << 16)) : 0u;
          }
          wf[y * 3 + i] = (v4u){dw[0], dw[1], dw[2], dw[3]};
        }
    }
    wg_barrier();
  }
  STAMP(2);

  const int G = lane >> 4, q4 = (lane >> 2) & 3, p4 = lane & 3, rg = 4 * (G & 1) + p4;
  const int wmain = (G >> 1) * R1_BLK + q4 * 64 + (rg & 1) * 32 + (rg >> 1) * 8;
  const int wtail = (G >> 1) * R1_BLK + 512 + ((rg >> 1) < 3 ? q4 * 48 + (rg >> 1) * 16 : 192 + q4 * 16) + (rg & 1) * 8;
  const int wq = R2_OFF + (G >> 1) * 256 + q4 * 64 + (rg & 1) * 32 + (rg >> 1) * 8;
  const int wo = L_WO + (G >> 1) * 1280 + ((G & 1) * 4 + q4) * 32 + p4 * 8;

#pragma unroll 1
  for (int L = 0; L < 4; ++L) {
    const int ty = L & 1;
    const u32 tagL = E + 1u + (u32)L;
    if (L == 0) {
      poll_flags(ws, lane < 16 ? flag_off(PH_F0, lane * 16 + j) : flag_off(PH_F0, tg * 16 + (lane & 15)), lane < 32, E + 1u);
      DRAIN();
      for (int i = w; i < 150; i += 4) dma(wsd, lane * 16u, WS_WQ + (u32)j * WIMG + (u32)i * 1024u, (u32)i * 1024u);
    } else {
      poll_flags(ws, flag_off(PH_X + L, tg * 16 + 2 * (lane & 7) + (w >> 1)), lane < 8, tagL);
    }
    STAMP(4 + L * 14 + 0);
    v2u csg[16];
    const u32 cso = WS_CS + (u32)(((tid >> 7) * 16 + j) * 16) * 1024u + (u32)(tid & 127) * 8u;
    if (L == 0) {
#pragma unroll
      for (int i = 0; i < 16; ++i) csg[i] = ld8(ws, cso + i * 1024u, 0);
    }
    SB();
    v4u x[40];
    {
      const u32 xb = WS_X + (u32)((L * 16 + tg) * 4 + w) * 40960u;
#pragma unroll
      for (int s = 0; s < 40; ++s) x[s] = ld16(ws, lane * 16u + (u32)(s & 3) * 1024u, xb + (u32)(s >> 2) * 4096u);
    }
    v4u st[4];
    const u32 sto = WS_ST + (u32)(((L * 16 + tg) * 4 + w) * 8 + 4 * h) * 512u + (u32)tok * 16u;
#pragma unroll
    for (int i = 0; i < 4; ++i) st[i] = ld16(ws, sto + i * 512u, 0);
    SB();
    VMWAIT(44);
    if (L == 0) {
      const bool pad = (tid & 63) >= 60;
      for (u32 sp = 0; sp < SPIN_CAP; ++sp) {
        bool ok = true;
#pragma unroll
        for (int i = 0; i < 16; ++i) ok &= csg[i].y == E + 1u;
        if (__all(ok || pad)) break;
        asm volatile("" ::: "memory");
#pragma unroll
        for (int i = 0; i < 16; ++i) csg[i] = ld8(ws, cso + i * 1024u, 0);
      }
      float s = 0.f;
#pragma unroll
      for (int i = 0; i < 16; ++i) s += __uint_as_float(csg[i].x);
      *(LAS float*)(L0 + L_CS + tid * 4) = pad ? 0.f : s;
    }
    wg_barrier();
    if (L == 0 && tid == 0) st4(E + 1u, ws, flag_off(PH_F1, cu), 0);
    STAMP(4 + L * 14 + 1);
    v16f aK = {}, aV = {}, aT = {}, aQ = {};
    v8bf f0, f1, f2;
    lptr bq = L0 + wq;
    asm volatile("" : "+v"(bq));
    {
      lptr m0 = L0 + wmain, m1 = m0 + 20 * R1_STEP, t0 = L0 + wtail, t1 = t0 + 20 * R1_STEP;
      asm volatile("" : "+v"(m0), "+v"(m1), "+v"(t0), "+v"(t1));
      f0 = trfrag(m0, m0 + 1408); f1 = trfrag(m0 + 256, m0 + 1664); f2 = trfrag(t0, t0 + 1408);
      SB();
#pragma unroll
      for (int s = 0; s < 40; ++s) {
        v8bf g0, g1, g2;
        if (s + 1 < 40) {
          const lptr mb = (s + 1 < 20 ? m0 : m1) + ((s + 1) % 20) * R1_STEP, tb = (s + 1 < 20 ? t0 : t1) + ((s + 1) % 20) * R1_STEP;
          g0 = trfrag(mb, mb + 1408); g1 = trfrag(mb + 256, mb + 1664); g2 = trfrag(tb, tb + 1408);
        }
        aK = mfma32(f0, asbf(x[s]), aK);
        aV = mfma32(f1, asbf(x[s]), aV);
        aT = mfma32(f2, asbf(x[s]), aT);
        if (s + 1 < 40) {
          __builtin_amdgcn_sched_group_barrier(0x8, 1, 0); __builtin_amdgcn_sched_group_barrier(0x100, 2, 0);
          __builtin_amdgcn_sched_group_barrier(0x8, 1, 0); __builtin_amdgcn_sched_group_barrier(0x100, 2, 0);
          __builtin_amdgcn_sched_group_barrier(0x8, 1, 0); __builtin_amdgcn_sched_group_barrier(0x100, 2, 0);
          f0 = g0; f1 = g1; f2 = g2;
        }
        SB();
      }
    }
    STAMP(4 + L * 14 + 2);
    for (u32 sp = 0; sp < SPIN_CAP; ++sp) {
      bool ok = true;
#pragma unroll
      for (int i = 0; i < 4; ++i) ok &= (st[i].y == tagL) & (st[i].w == tagL);
      if (__all(ok)) break;
      asm volatile("" ::: "memory");
#pragma unroll
      for (int i = 0; i < 4; ++i) st[i] = ld16(ws, sto + i * 512u, 0);
    }
    float mean, rstd;
    {
      float s1 = (__uint_as_float(st[0].x) + __uint_as_float(st[1].x)) + (__uint_as_float(st[2].x) + __uint_as_float(st[3].x));
      float s2 = (__uint_as_float(st[0].z) + __uint_as_float(st[1].z)) + (__uint_as_float(st[2].z) + __uint_as_float(st[3].z));
      auto r1 = __builtin_amdgcn_permlane32_swap(__float_as_uint(s1), __float_as_uint(s1), false, false);
      auto r2 = __builtin_amdgcn_permlane32_swap(__float_as_uint(s2), __float_as_uint(s2), false, false);
      s1 = __uint_as_float(r1[0]) + __uint_as_float(r1[1]);
      s2 = __uint_as_float(r2[0]) + __uint_as_float(r2[1]);
      mean = s1 * (1.f / 640.f);
      rstd = __builtin_amdgcn_rsqf(fmaxf(s2 * (1.f / 640.f) - mean * mean, 0.f) + 1e-5f);
    }
    const LAS v4f* cs = (const LAS v4f*)(L0 + L_CS + (ty * 2 + h) * 256);
    float kk[20], vv[20];
#pragma unroll
    for (int i = 0; i < 4; ++i) {
      const v4f ck = cs[i], cv = cs[4 + i];
#pragma unroll
      for (int t = 0; t < 4; ++t) {
        kk[4 * i + t] = __builtin_fmaf(-mean, ck[t], aK[4 * i + t]) * rstd;
        vv[4 * i + t] = __builtin_fmaf(-mean, cv[t], aV[4 * i + t]) * rstd;
      }
    }
    {
      const v4f ck = cs[12], cv = cs[13];
#pragma unroll
      for (int t = 0; t < 4; ++t) {
        kk[16 + t] = __builtin_fmaf(-mean, ck[t], aT[t]) * rstd;
        vv[16 + t] = __builtin_fmaf(-mean, cv[t], aT[4 + t]) * rstd;
      }
    }
    u32 kp[10];
#pragma unroll
    for (int i = 0; i < 10; ++i) kp[i] = pk2(kk[2 * i], kk[2 * i + 1]);
    if (ty == 0) {
      wg_barrier();
      const int row = h * 144 + 16 + 32 * w + tok;
      LAS v4f* vp = (LAS v4f*)(L0 + L_VP + row * 80);
      LAS v4u* kq = (LAS v4u*)(L0 + L_KP + row * 48);
#pragma unroll
      for (int i = 0; i < 5; ++i) vp[i] = (v4f){vv[4 * i], vv[4 * i + 1], vv[4 * i + 2], vv[4 * i + 3]};
      kq[0] = (v4u){kp[0], kp[1], kp[2], kp[3]}; kq[1] = (v4u){kp[4], kp[5], kp[6], kp[7]}; kq[2] = (v4u){kp[8], kp[9], 0u, 0u};
      if (w == 3 && tg < 15 && tok >= 16) {
        const u32 hb = WS_HK + (u32)((L >> 1) * 256 + cu) * 4096u, ho = (u32)(h * 16 + tok - 16) * 16u;
#pragma unroll
        for (int i = 0; i < 5; ++i)
          st16((v4u){__float_as_uint(vv[4 * i]), __float_as_uint(vv[4 * i + 1]), __float_as_uint(vv[4 * i + 2]), __float_as_uint(vv[4 * i + 3])}, ws, ho + i * 512u, hb);
        st16((v4u){kp[0], kp[1], kp[2], kp[3]}, ws, ho + 2560u, hb);
        st16((v4u){kp[4], kp[5], kp[6], kp[7]}, ws, ho + 3072u, hb);
        st16((v4u){kp[8], kp[9], 0u, 0u}, ws, ho + 3584u, hb);
      }
      if (w == 3 && tg < 15) { DRAIN(); if (lane == 0) st4(tagL, ws, flag_off(PH_M + L, cu), 0); }
    } else {
      float pv[20];
      u32 phi[20];
      feat_proj(kp, wf, pv);
      dual_softmax(pv, phi);
      wg_barrier();
      const int row = h * 128 + 32 * w + tok, sw = (tok >> 1) & 3;
      const lptr pa = L0 + L_PA + row * 64, pb = L0 + L_PB + row * 64;
#pragma unroll
      for (int i = 0; i < 4; ++i) *(LAS v4u*)(pa + ((i ^ sw) * 16)) = (v4u){phi[4 * i], phi[4 * i + 1], phi[4 * i + 2], phi[4 * i + 3]};
      *(LAS v4u*)(pb + ((0 ^ sw) * 16)) = (v4u){pk2(vv[0], vv[1]), pk2(vv[2], vv[3]), pk2(vv[4], vv[5]), pk2(vv[6], vv[7])};
      *(LAS v4u*)(pb + ((1 ^ sw) * 16)) = (v4u){pk2(vv[8], vv[9]), pk2(vv[10], vv[11]), pk2(vv[12], vv[13]), pk2(vv[14], vv[15])};
      *(LAS v4u*)(pb + ((2 ^ sw) * 16)) = (v4u){pk2(vv[16], vv[17]), pk2(vv[18], vv[19]), 0x3f80u, 0u};
      *(LAS v4u*)(pb + ((3 ^ sw) * 16)) = (v4u){phi[16], phi[17], phi[18], phi[19]};
      wg_barrier();
      const int hd = w >> 1, part = w & 1, mm = rg >> 1, hh = rg & 1;
      const int trow = (hd * 128 + 8 * (G >> 1) + q4) * 64, s0 = q4 >> 1, s1 = (2 + s0) & 3;
      const int ca = (2 * hh + (mm >> 1)) ^ (part ? 3 : 0);
      const lptr a0 = L0 + (part ? L_PB : L_PA) + trow + ((ca ^ s0) * 16) + (mm & 1) * 8;
      const lptr a1 = L0 + (part ? L_PB : L_PA) + trow + 256 + ((ca ^ s1) * 16) + (mm & 1) * 8;
      const lptr b0 = L0 + L_PB + trow + (((rg >> 1) ^ s0) * 16) + (rg & 1) * 8;
      const lptr b1 = L0 + L_PB + trow + 256 + (((rg >> 1) ^ s1) * 16) + (rg & 1) * 8;
      v16f kv = {};
#pragma unroll
      for (int s = 0; s < 8; ++s) kv = mfma32(trfrag(a0 + s * 1024, a1 + s * 1024), trfrag(b0 + s * 1024, b1 + s * 1024), kv);
      const u32 kb = WS_KV + (u32)(((L >> 1) * 16 + j) * 16 + tg) * 3584u;
      if (tok < 21 && (part == 0 || h == 0)) {
        const u32 o0 = (u32)((hd * 5 + (part ? 4 : 2 * h)) * 21 + tok) * 16u;
        st16((v4u){pk2(kv[0], kv[1]), pk2(kv[2], kv[3]), pk2(kv[4], kv[5]), pk2(kv[6], kv[7])}, ws, o0, kb);
        if (part == 0) st16((v4u){pk2(kv[8], kv[9]), pk2(kv[10], kv[11]), pk2(kv[12], kv[13]), pk2(kv[14], kv[15])}, ws, o0 + 336u, kb);
      }
      DRAIN();
      wg_barrier();
      if (tid == 0) st4(tagL, ws, flag_off(PH_M + L, cu), 0);
    }
    STAMP(4 + L * 14 + 3);
    {
      f0 = trfrag(bq, bq + 512); f1 = trfrag(bq + 1024, bq + 1536); f2 = trfrag(bq + 2048, bq + 2560);
      SB();
#pragma unroll
      for (int s = 0; s < 40; ++s) {
        v8bf gq;
        if (s + 3 < 40) gq = trfrag(bq + (s + 3) * 1024, bq + (s + 3) * 1024 + 512);
        aQ = mfma32(f0, asbf(x[s]), aQ);
        if (s + 3 < 40) { __builtin_amdgcn_sched_group_barrier(0x8, 1, 0); __builtin_amdgcn_sched_group_barrier(0x100, 2, 0); }
        f0 = f1; f1 = f2; f2 = gq;
        SB();
      }
    }
    STAMP(4 + L * 14 + 4);
    u32 qp[10];
    {
      float qq[20];
#pragma unroll
      for (int i = 0; i < 4; ++i) {
        const v4f c = cs[8 + i];
#pragma unroll
        for (int t = 0; t < 4; ++t) qq[4 * i + t] = __builtin_fmaf(-mean, c[t], aQ[4 * i + t]) * rstd;
      }
      const v4f c = cs[14];
#pragma unroll
      for (int t = 0; t < 4; ++t) qq[16 + t] = __builtin_fmaf(-mean, c[t], aT[8 + t]) * rstd;
#pragma unroll
      for (int i = 0; i < 10; ++i) qp[i] = pk2(qq[2 * i], qq[2 * i + 1]);
    }
    float oo[20];
    if (ty == 0) {
      if (w == 0) {
        v4u hk[4];
        if (tg > 0) {
          poll_flags(ws, flag_off(PH_M + L, cu - 16), lane == 0, tagL);
#pragma unroll
          for (int i = 0; i < 4; ++i) hk[i] = ld16(ws, lane * 16u + i * 1024u, WS_HK + (u32)((L >> 1) * 256 + cu - 16) * 4096u);
        } else {
#pragma unroll
          for (int i = 0; i < 4; ++i) hk[i] = (v4u){0u, 0u, 0u, 0u};
        }
        const int row = ((lane >> 4) & 1) * 144 + (lane & 15);
#pragma unroll
        for (int i = 0; i < 4; ++i) {
          const int c = 2 * i + h;
          *(LAS v4u*)(L0 + (c < 5 ? L_VP + row * 80 + c * 16 : L_KP + row * 48 + (c - 5) * 16)) = hk[i];
        }
      }
      wg_barrier();
      STAMP(4 + L * 14 + 5);
      if (L == 0) poll_flags(ws, flag_off(PH_F1, (lane >> 1) * 16 + 2 * cg + (lane & 1)), lane < 20, E + 1u);
      for (int i = w; i < 100; i += 4) dma(wsd, lane * 16u, WS_WO + (u32)cg * 102400u + (u32)i * 1024u, (u32)(L_WO + i * 1024));
      const int row = h * 144 + 16 + 32 * w + tok;
      const lptr kr = L0 + L_KP + row * 48, vr = L0 + L_VP + row * 80;
      float sc[8];
#pragma unroll
      for (int i = 0; i < 8; ++i) {
        const v4u k0 = *(LAS v4u*)(kr - i * 96), k1 = *(LAS v4u*)(kr - i * 96 + 16);
        const v2u k2 = *(LAS v2u*)(kr - i * 96 + 32);
        float a = dot2(qp[0], k0.x, 0.f), c = dot2(qp[1], k0.y, 0.f);
        a = dot2(qp[2], k0.z, a); c = dot2(qp[3], k0.w, c);
        a = dot2(qp[4], k1.x, a); c = dot2(qp[5], k1.y, c);
        a = dot2(qp[6], k1.z, a); c = dot2(qp[7], k1.w, c);
        a = dot2(qp[8], k2.x, a); c = dot2(qp[9], k2.y, c);
        sc[i] = (a + c) * 0.22360679774997896f;
        if (128 * tg + 32 * w + tok - 2 * i < 0) sc[i] = -1e9f;
      }
      float mx = sc[0];
#pragma unroll
      for (int i = 1; i < 8; ++i) mx = fmaxf(mx, sc[i]);
      const float C = 1.4426950408889634f, mc = mx * C;
      float sum = 0.f;
#pragma unroll
      for (int i = 0; i < 8; ++i) { sc[i] = __builtin_amdgcn_exp2f(__builtin_fmaf(sc[i], C, -mc)); sum += sc[i]; }
      const float rs = __builtin_amdgcn_rcpf(sum);
      v2f o2[10];
#pragma unroll
      for (int e = 0; e < 10; ++e) o2[e] = (v2f){0.f, 0.f};
#pragma unroll
      for (int i = 0; i < 8; ++i) {
        const float pi = sc[i] * rs;
        const v2f pp = {pi, pi};
#pragma unroll
        for (int c = 0; c < 5; ++c) {
          const v4f v = *(LAS v4f*)(vr - i * 160 + c * 16);
          o2[2 * c] = __builtin_elementwise_fma(pp, (v2f){v.x, v.y}, o2[2 * c]);
          o2[2 * c + 1] = __builtin_elementwise_fma(pp, (v2f){v.z, v.w}, o2[2 * c + 1]);
        }
      }
#pragma unroll
      for (int e = 0; e < 10; ++e) { oo[2 * e] = o2[e].x; oo[2 * e + 1] = o2[e].y; }
    } else {
      float pv[20];
      u32 phi[20];
      feat_proj(qp, wf, pv);
      dual_softmax(pv, phi);
      wg_barrier();
      STAMP(4 + L * 14 + 5);
      for (int i = w; i < 100; i += 4) dma(wsd, lane * 16u, WS_WO + (u32)(8 + cg) * 102400u + (u32)i * 1024u, (u32)(L_WO + i * 1024));
      poll_flags(ws, flag_off(PH_M + L, (lane & 15) * 16 + j), lane < 16, tagL);
      STAMP(4 + L * 14 + 6);
      if (tid < 210) {
        v4u pr[16];
        const u32 kb = WS_KV + (u32)(((L >> 1) * 16 + j) * 16) * 3584u;
#pragma unroll
        for (int i = 0; i < 16; ++i) pr[i] = ld16(ws, tid * 16u, kb + i * 3584u);
        SB();
        float a[8];
#pragma unroll
        for (int c = 0; c < 4; ++c) { a[2 * c] = bflo(pr[0][c]); a[2 * c + 1] = bfhi(pr[0][c]); }
#pragma unroll
        for (int i = 1; i < 16; ++i)
#pragma unroll
          for (int c = 0; c < 4; ++c) { a[2 * c] += bflo(pr[i][c]); a[2 * c + 1] += bfhi(pr[i][c]); }
        *(LAS v4u*)(L0 + L_KVR + (tid / 21) * 352 + (tid % 21) * 16) = (v4u){pk2(a[0], a[1]), pk2(a[2], a[3]), pk2(a[4], a[5]), pk2(a[6], a[7])};
      }
      wg_barrier();
      STAMP(4 + L * 14 + 7);
      const int m = tok >> 3, hp = (tok >> 2) & 1, t = tok & 3;
      const int ex = hp == h ? 4 * m + t : 21;
      const int ey = (hp == h && (m == 0 || (m == 1 && t == 0))) ? 16 + 4 * m + t : 21;
      const lptr kx = L0 + L_KVR + h * 1760 + ex * 16, ky = L0 + L_KVR + h * 1760 + ey * 16;
      v16f X = {}, Y = {};
#pragma unroll
      for (int i = 0; i < 5; ++i) {
        const v8bf bfr = asbf((v4u){phi[4 * i], phi[4 * i + 1], phi[4 * i + 2], phi[4 * i + 3]});
        X = mfma32(asbf(*(LAS v4u*)(kx + i * 352)), bfr, X);
        Y = mfma32(asbf(*(LAS v4u*)(ky + i * 352)), bfr, Y);
      }
      const float rd = __builtin_amdgcn_rcpf(Y[4] + 1e-6f);
#pragma unroll
      for (int i = 0; i < 16; ++i) oo[i] = X[i] * rd;
#pragma unroll
      for (int i = 0; i < 4; ++i) oo[16 + i] = Y[i] * rd;
    }
    {
      const u32 ob = WS_O + (u32)((L * 16 + tg) * 8 + 2 * w) * 20480u + (u32)(5 * j) * 256u;
      const u32 lo = (u32)(tok >> 4) * 20480u + (u32)(tok & 15) * 16u;
      st16((v4u){pk2(oo[0], oo[1]), pk2(oo[2], oo[3]), pk2(oo[4], oo[5]), pk2(oo[6], oo[7])}, ws, lo + (u32)h * 768u, ob);
      st16((v4u){pk2(oo[8], oo[9]), pk2(oo[10], oo[11]), pk2(oo[12], oo[13]), pk2(oo[14], oo[15])}, ws, lo + (u32)h * 768u + 256u, ob);
      st8((v2u){pk2(oo[16], oo[17]), pk2(oo[18], oo[19])}, ws, lo + 512u + (u32)h * 8u, ob);
    }
    STAMP(4 + L * 14 + 8);
    DRAIN();
    wg_barrier();
    if (tid == 0) st4(tagL, ws, flag_off(PH_O + L, cu), 0);
    STAMP(4 + L * 14 + 9);
    poll_flags(ws, flag_off(PH_O + L, tg * 16 + (lane & 15)), lane < 16, tagL);
    STAMP(4 + L * 14 + 10);
    v4u of[20];
    {
      const u32 ob = WS_O + (u32)((L * 16 + tg) * 8 + th * 4 + w) * 20480u;
#pragma unroll
      for (int s = 0; s < 20; ++s) of[s] = ld16(ws, lane * 16u + (u32)(s & 3) * 1024u, ob + (u32)(s >> 2) * 4096u);
    }
    SB();
    v4f ac[5];
    {
      lptr w0 = L0 + wo, w1 = w0 + 10 * 5120;
      asm volatile("" : "+v"(w0), "+v"(w1));
      v8bf fa[5];
#pragma unroll
      for (int c = 0; c < 5; ++c) { fa[c] = trfrag(w0 + c * 256, w0 + 2560 + c * 256); ac[c] = (v4f){0.f, 0.f, 0.f, 0.f}; }
      SB();
#pragma unroll
      for (int s = 0; s < 20; ++s) {
        v8bf ga[5];
        if (s + 1 < 20) {
          const lptr wb = (s + 1 < 10 ? w0 : w1) + ((s + 1) % 10) * 5120;
#pragma unroll
          for (int c = 0; c < 5; ++c) ga[c] = trfrag(wb + c * 256, wb + 2560 + c * 256);
        }
#pragma unroll
        for (int c = 0; c < 5; ++c) ac[c] = mfma16(fa[c], asbf(of[s]), ac[c]);
        if (s + 1 < 20) {
#pragma unroll
          for (int c = 0; c < 5; ++c) { __builtin_amdgcn_sched_group_barrier(0x8, 1, 0); __builtin_amdgcn_sched_group_barrier(0x100, 2, 0); fa[c] = ga[c]; }
        }
        SB();
      }
    }
    STAMP(4 + L * 14 + 11);
    v2u xr[5];
#pragma unroll
    for (int c = 0; c < 5; ++c) {
      xr[c].x = pk2(res[4 * c] + ac[c][0], res[4 * c + 1] + ac[c][1]);
      xr[c].y = pk2(res[4 * c + 2] + ac[c][2], res[4 * c + 3] + ac[c][3]);
    }
    if (L == 3) {
      GAS char* op = (GAS char*)p.out + ((128 * tg + 64 * th + 16 * w + t16) * 1280 + (80 * cg + 4 * g) * 2);
#pragma unroll
      for (int c = 0; c < 5; ++c) *(GAS v2u*)(op + 32 * c) = xr[c];
      if (b == 0 && tid == 0) st4(E + 16u, ws, 0, 0);
    } else {
      const u32 xb = WS_X + (u32)(((L + 1) * 16 + tg) * 4 + th * 2 + (w >> 1)) * 40960u + (u32)(5 * cg) * 1024u;
      const u32 tk = (u32)((w & 1) * 16 + t16);
#pragma unroll
      for (int c = 0; c < 4; c += 2) {
        auto r0 = __builtin_amdgcn_permlane32_swap(xr[c].x, xr[c + 1].x, false, false);
        auto r1 = __builtin_amdgcn_permlane32_swap(xr[c].y, xr[c + 1].y, false, false);
        st16((v4u){r0[0], r1[0], r0[1], r1[1]}, ws, (u32)(c + h) * 1024u + ((u32)(g & 1) * 32u + tk) * 16u, xb);
      }
      st8(xr[4], ws, 4096u + ((u32)(g & 1) * 32u + tk) * 16u + (u32)(g >> 1) * 8u, xb);
      float s1 = 0.f, s2 = 0.f;
#pragma unroll
      for (int c = 0; c < 5; ++c) {
        res[4 * c] = bflo(xr[c].x); res[4 * c + 1] = bfhi(xr[c].x); res[4 * c + 2] = bflo(xr[c].y); res[4 * c + 3] = bfhi(xr[c].y);
      }
#pragma unroll
      for (int i = 0; i < 20; ++i) { s1 += res[i]; s2 = __builtin_fmaf(res[i], res[i], s2); }
      s1 += __shfl_xor(s1, 16); s2 += __shfl_xor(s2, 16);
      s1 += __shfl_xor(s1, 32); s2 += __shfl_xor(s2, 32);
      if (lane < 16)
        st16((v4u){__float_as_uint(s1), tagL + 1u, __float_as_uint(s2), tagL + 1u}, ws, tk * 16u,
             WS_ST + (u32)((((L + 1) * 16 + tg) * 4 + th * 2 + (w >> 1)) * 8 + cg) * 512u);
      DRAIN();
      wg_barrier();
      if (tid == 0) st4(tagL + 1u, ws, flag_off(PH_X + L + 1, cu), 0);
      STAMP(4 + L * 14 + 12);
      if (L == 0) poll_flags(ws, flag_off(PH_F1, (lane & 15) * 16 + j), lane < 16, E + 1u);
      for (int i = w; i < 150; i += 4) dma(wsd, lane * 16u, WS_WQ + (u32)((ty ^ 1) * 16 + j) * WIMG + (u32)i * 1024u, (u32)i * 1024u);
    }
    STAMP(4 + L * 14 + 13);
  }
}

extern "C" void kernel_launch(void* const* d_in, const int* in_sizes, int n_in, void* d_out, int out_size, void* d_ws,
                              size_t ws_size, hipStream_t stream) {
  Params p;
  p.x0 = (const u16*)d_in[0]; p.wqA = (const u16*)d_in[1]; p.woA = (const u16*)d_in[2];
  p.wqB = (const u16*)d_in[4]; p.wphi = (const u16*)d_in[5]; p.woB = (const u16*)d_in[6];
  p.out = (u16*)d_out; p.ws = (unsigned char*)d_ws;
  (void)hipFuncSetAttribute((const void*)fused_kernel, hipFuncAttributeMaxDynamicSharedMemorySize, LDS_BYTES);
  fused_kernel<<<256, 256, LDS_BYTES, stream>>>(p);
}
```

```cpp
#include <hip/hip_runtime.h>
#include <hip/hip_bf16.h>

typedef unsigned short u16;
typedef unsigned u32;
typedef unsigned long long u64;
typedef u32 v4u __attribute__((ext_vector_type(4)));
typedef u32 v2u __attribute__((ext_vector_type(2)));
typedef int v4i __attribute__((ext_vector_type(4)));
typedef float v4f __attribute__((ext_vector_type(4)));
typedef float v2f __attribute__((ext_vector_type(2)));
typedef float v16f __attribute__((ext_vector_type(16)));
typedef __bf16 v8bf __attribute__((ext_vector_type(8)));
typedef __bf16 v4bf __attribute__((ext_vector_type(4)));
typedef __bf16 v2bf __attribute__((ext_vector_type(2)));
#define LAS __attribute__((address_space(3)))
#define GAS __attribute__((address_space(1)))
typedef LAS char* lptr;

#define PROF 0

constexpr u32 WS_FLAG = 4096;
constexpr u32 WS_CS = WS_FLAG + 16 * 256 * 128;
constexpr u32 WS_ST = WS_CS + 2 * 16 * 16 * 1024;
constexpr u32 WS_HK = WS_ST + 4 * 16 * 4 * 8 * 512;
constexpr u32 WS_KV = WS_HK + 2 * 256 * 4096;
constexpr u32 WS_WQ = WS_KV + 2 * 256 * 3584;
constexpr u32 WS_WO = WS_WQ + 2 * 16 * 153600;
constexpr u32 WS_X = WS_WO + 2 * 8 * 102400;
constexpr u32 WS_O = WS_X + 5 * 64 * 40960;
constexpr u32 WS_DBG = WS_O + 5 * 128 * 20480;
constexpr u32 WS_END = WS_DBG + 256 * 4 * 64 * 8;
constexpr int R1_STEP = 2816, R2_OFF = 112640, WIMG = 153600;
constexpr int L_CS = 157696;
constexpr int L_KVR = L_CS + 1024;
constexpr int L_WPHI = L_KVR + 3520;
constexpr int L_WO = 36864;
constexpr int L_VP = 0, L_KP = 23040;
constexpr int L_PA = 0, L_PB = 16384;
constexpr int LDS_BYTES = 163840;
constexpr int AUX_SC1 = 16;
constexpr u32 OOB = 0x80000000u;
constexpr u32 SPIN_CAP = 1u << 16;
enum { PH_F0 = 0, PH_F1 = 1, PH_O = 2, PH_X = 6, PH_M = 10 };

struct Params {
  const u16* x0; const u16* wqA; const u16* woA; const u16* wqB; const u16* wphi; const u16* woB;
  u16* out; unsigned char* ws;
};

typedef __amdgpu_buffer_rsrc_t rsrc_t;
__device__ __forceinline__ rsrc_t mk_rsrc(const void* p, u32 bytes) { return __builtin_amdgcn_make_buffer_rsrc((void*)p, 0, bytes, 0x00020000); }
__device__ __forceinline__ v4u ld16(rsrc_t r, u32 vo, u32 so) { return __builtin_amdgcn_raw_buffer_load_b128(r, vo, so, AUX_SC1); }
__device__ __forceinline__ v2u ld8(rsrc_t r, u32 vo, u32 so) { return __builtin_amdgcn_raw_buffer_load_b64(r, vo, so, AUX_SC1); }
__device__ __forceinline__ u32 ld4(rsrc_t r, u32 vo, u32 so) { return __builtin_amdgcn_raw_buffer_load_b32(r, vo, so, AUX_SC1); }
__device__ __forceinline__ v4u ld16p(rsrc_t r, u32 vo, u32 so) { return __builtin_amdgcn_raw_buffer_load_b128(r, vo, so, 0); }
__device__ __forceinline__ v2u ld8p(rsrc_t r, u32 vo, u32 so) { return __builtin_amdgcn_raw_buffer_load_b64(r, vo, so, 0); }
__device__ __forceinline__ void st16(v4u v, rsrc_t r, u32 vo, u32 so) { __builtin_amdgcn_raw_buffer_store_b128(v, r, vo, so, AUX_SC1); }
__device__ __forceinline__ void st8(v2u v, rsrc_t r, u32 vo, u32 so) { __builtin_amdgcn_raw_buffer_store_b64(v, r, vo, so, AUX_SC1); }
__device__ __forceinline__ void st4(u32 v, rsrc_t r, u32 vo, u32 so) { __builtin_amdgcn_raw_buffer_store_b32(v, r, vo, so, AUX_SC1); }
#define DRAIN() asm volatile("s_waitcnt vmcnt(0)" ::: "memory")
#define VMWAIT(n) asm volatile("s_waitcnt vmcnt(" #n ")" ::: "memory")
#define SB() __builtin_amdgcn_sched_barrier(0)
__device__ __forceinline__ void wg_barrier() {
  asm volatile("s_waitcnt lgkmcnt(0)\n\ts_barrier" ::: "memory");
}
template <int I> struct IC { static constexpr int value = I; };
template <int I, int N, class F> __device__ __forceinline__ void sfor(F&& f) {
  if constexpr (I < N) { f(IC<I>{}); sfor<I + 1, N>(f); }
}
#define A10(p) "a" #p "0", "a" #p "1", "a" #p "2", "a" #p "3", "a" #p "4", "a" #p "5", "a" #p "6", "a" #p "7", "a" #p "8", "a" #p "9"
#define ACC_OWNED "a96", "a97", "a98", "a99", A10(10), A10(11), A10(12), A10(13), A10(14), A10(15), A10(16), A10(17), A10(18), A10(19), \
  A10(20), A10(21), A10(22), A10(23), A10(24), "a250", "a251", "a252", "a253", "a254", "a255"
constexpr int AX = 96;
template <int S> __device__ __forceinline__ void ld_frag(v4i rs, u32 voff, u32 sbase) {
  u32 t;
  asm volatile("s_add_u32 %0, %3, %c6\n\tbuffer_load_dwordx4 a[%c4:%c5], %1, %2, %0 offen sc1"
               : "=&s"(t) : "v"(voff), "s"(rs), "s"(sbase), "i"(AX + 4 * S), "i"(AX + 4 * S + 3), "i"(S * 1024) : "scc", ACC_OWNED);
}
template <int N> __device__ __forceinline__ void vmwait() { asm volatile("s_waitcnt vmcnt(%c0)" :: "i"(N) : "memory"); }
template <int S, bool FIRST> __device__ __forceinline__ void mfma32_x(v16f& c, v8bf a) {
  if constexpr (FIRST) asm volatile("v_mfma_f32_32x32x16_bf16 %0, %1, a[%c2:%c3], 0" : "=&v"(c) : "v"(a), "i"(AX + 4 * S), "i"(AX + 4 * S + 3));
  else asm volatile("v_mfma_f32_32x32x16_bf16 %0, %1, a[%c2:%c3], %0" : "+v"(c) : "v"(a), "i"(AX + 4 * S), "i"(AX + 4 * S + 3));
}
template <int S, bool FIRST> __device__ __forceinline__ void mfma16_x(v4f& c, v8bf a) {
  if constexpr (FIRST) asm volatile("v_mfma_f32_16x16x32_bf16 %0, %1, a[%c2:%c3], 0" : "=&v"(c) : "v"(a), "i"(AX + 4 * S), "i"(AX + 4 * S + 3));
  else asm volatile("v_mfma_f32_16x16x32_bf16 %0, %1, a[%c2:%c3], %0" : "+v"(c) : "v"(a), "i"(AX + 4 * S), "i"(AX + 4 * S + 3));
}
template <bool FIRST, class TA, class TB> __device__ __forceinline__ void mfma32_v(v16f& c, TA a, TB b) {
  if constexpr (FIRST) asm volatile("s_nop 1\n\tv_mfma_f32_32x32x16_bf16 %0, %1, %2, 0" : "=&v"(c) : "v"(a), "v"(b));
  else asm volatile("s_nop 1\n\tv_mfma_f32_32x32x16_bf16 %0, %1, %2, %0" : "+v"(c) : "v"(a), "v"(b));
}
#define MFMA_DONE1(x) asm volatile("s_nop 11" : "+v"(x))
#define MFMA_DONE2(x, y) asm volatile("s_nop 11" : "+v"(x), "+v"(y))
#define MFMA_DONE3(x, y, z) asm volatile("s_nop 11" : "+v"(x), "+v"(y), "+v"(z))
template <int Q> __device__ __forceinline__ void dma(v4i rs, u32 voff, u32 img, u32 wo) {
  u32 t;
  asm volatile("s_add_u32 m0, %4, %c5\n\ts_add_u32 %0, %3, %c5\n\tbuffer_load_dwordx4 %1, %2, %0 offen sc1 lds"
               : "=&s"(t) : "v"(voff), "s"(rs), "s"(img), "s"(wo), "i"(Q * 1024) : "scc");
}
template <int BLK, int HALF> __device__ __forceinline__ void dma_x0(v4i rs, u32 voff, u32 ldsw) {
  asm volatile("s_add_u32 m0, %2, %c3\n\ts_nop 0\n\tbuffer_load_dwordx4 %0, %1, 0 offen offset:%c4 lds"
               :: "v"(voff), "s"(rs), "s"(ldsw), "i"(BLK * 2048 + HALF * 1024 - BLK * 64), "i"(BLK * 64) : "scc");
}
template <int R, int OFF> __device__ __forceinline__ void lds_to_acc8(lptr a) {
  asm volatile("ds_read_b64 a[%c1:%c2], %0 offset:%c3" :: "v"(a), "i"(R), "i"(R + 1), "i"(OFF) : ACC_OWNED);
}
template <int R, int OFF> __device__ __forceinline__ void mem_to_acc8(v4i rs, u32 voff) {
  asm volatile("buffer_load_dwordx2 a[%c2:%c3], %0, %1, 0 offen offset:%c4" :: "v"(voff), "s"(rs), "i"(R), "i"(R + 1), "i"(OFF) : ACC_OWNED);
}
template <int I> __device__ __forceinline__ void park_load(v4i rs, u32 voff, u32 img) {
  u32 t;
  asm volatile("s_add_u32 %0, %3, %c6\n\tbuffer_load_dwordx4 a[%c4:%c5], %1, %2, %0 offen sc1"
               : "=&s"(t) : "v"(voff), "s"(rs), "s"(img), "i"(176 + 4 * I), "i"(179 + 4 * I), "i"((36 + 4 * I) * 1024) : "scc", ACC_OWNED);
}
template <int I> __device__ __forceinline__ void park_store(lptr a) {
  asm volatile("ds_write_b128 %0, a[%c1:%c2] offset:%c3" :: "v"(a), "i"(176 + 4 * I), "i"(179 + 4 * I), "i"((I % 10) * 4096) : "memory");
}
__device__ __forceinline__ void ld16_asm(v4u& d, v4i rs, u32 voff, u32 soff) {
  asm volatile("buffer_load_dwordx4 %0, %1, %2, %3 offen sc1" : "=v"(d) : "v"(voff), "s"(rs), "s"(soff));
}
constexpr int p1_first(int s) { return s < 32 ? 1 : s < 38 ? 1 : 0; }
constexpr int p1_second(int s) { return (s >= 12 && s <= 22 && !(s & 1)) ? 1 : (s >= 24 && s < 28) ? 1 : 0; }
constexpr int p1_younger(int s) {
  int n = s < 8 ? 7 - s : p1_second(s - 8);
  for (int t = (s < 8 ? 0 : s - 7); t < s; ++t) n += p1_first(t) + p1_second(t);
  return n;
}
constexpr int pp_ops(int s) { return s < 12 ? 1 : s < 18 ? 2 : 0; }
constexpr int pp_younger(int s) {
  int n = s < 8 ? 7 - s : 0;
  for (int t = (s < 8 ? 0 : s - 7); t < s; ++t) n += pp_ops(t);
  return n;
}
__device__ __forceinline__ u32 pk2(float a, float b) { v2f f = {a, b}; return __builtin_bit_cast(u32, __builtin_convertvector(f, v2bf)); }
__device__ __forceinline__ float bflo(u32 d) { return __uint_as_float(d << 16); }
__device__ __forceinline__ float bfhi(u32 d) { return __uint_as_float(d & 0xffff0000u); }
__device__ __forceinline__ v8bf asbf(v4u v) { return __builtin_bit_cast(v8bf, v); }
__device__ __forceinline__ v8bf trfrag(lptr p0, lptr p1) {
  v4bf a = __builtin_amdgcn_ds_read_tr16_b64_v4bf16((LAS v4bf*)p0);
  v4bf b = __builtin_amdgcn_ds_read_tr16_b64_v4bf16((LAS v4bf*)p1);
  return __builtin_shufflevector(a, b, 0, 1, 2, 3, 4, 5, 6, 7);
}
__device__ __forceinline__ v16f mfma32(v8bf a, v8bf b, v16f c) { return __builtin_amdgcn_mfma_f32_32x32x16_bf16(a, b, c, 0, 0, 0); }
__device__ __forceinline__ v4f mfma16(v8bf a, v8bf b, v4f c) { return __builtin_amdgcn_mfma_f32_16x16x32_bf16(a, b, c, 0, 0, 0); }
__device__ __forceinline__ float dot2(u32 a, u32 b, float c) {
  return __builtin_amdgcn_fdot2_f32_bf16(__builtin_bit_cast(v2bf, a), __builtin_bit_cast(v2bf, b), c, false);
}
__device__ __forceinline__ void poll_flags(rsrc_t ws, u32 voff, bool on, u32 tag) {
  for (u32 sp = 0; sp < SPIN_CAP; ++sp) {
    u32 v = tag;
    asm volatile("" ::: "memory");
    if (on) v = ld4(ws, voff, 0);
    if (__all(v == tag)) break;
  }
  __builtin_amdgcn_fence(__ATOMIC_ACQUIRE, "wavefront");
}
__device__ __forceinline__ u32 flag_off(int ph, int cu) { return WS_FLAG + (u32)(ph * 256 + cu) * 128u; }

#define STAMP(i) do {} while (0)

constexpr u32 SENT = 0xffffffffu;
__device__ __forceinline__ void probe(rsrc_t ws, u32 voff, bool on) {
  for (u32 sp = 0; sp < SPIN_CAP; ++sp) {
    u32 v = 0;
    asm volatile("" ::: "memory");
    if (on) v = ld4(ws, voff, 0);
    if (__all(v != SENT)) break;
  }
  __builtin_amdgcn_fence(__ATOMIC_ACQUIRE, "wavefront");
}
__device__ __forceinline__ u32 max4(v4u v) { return max(max(v.x, v.y), max(v.z, v.w)); }
__device__ __forceinline__ void strict_wait(rsrc_t ws, u32 voff, u32 base, int n) {
  for (int s = 0; s < n; ++s)
    for (u32 sp = 0; sp < SPIN_CAP; ++sp) {
      asm volatile("" ::: "memory");
      if (__all(max4(ld16(ws, voff, base + (u32)s * 1024u)) != SENT)) break;
    }
  __builtin_amdgcn_fence(__ATOMIC_ACQUIRE, "wavefront");
}
__device__ __forceinline__ v2f pkfma(v2f a, v2f b, v2f c) { return __builtin_elementwise_fma(a, b, c); }
__device__ __forceinline__ float max3f(float a, float b, float c) { float r; asm("v_max3_f32 %0, %1, %2, %3" : "=v"(r) : "v"(a), "v"(b), "v"(c)); return r; }
__device__ __forceinline__ float min3f(float a, float b, float c) { float r; asm("v_min3_f32 %0, %1, %2, %3" : "=v"(r) : "v"(a), "v"(b), "v"(c)); return r; }
__device__ __forceinline__ void dual_softmax(const v2f (&pv)[10], u32 (&phi)[20]) {
  const float C = 1.4426950408889634f;
  float mx = max3f(pv[0].x, pv[0].y, pv[0].y), mn = min3f(pv[0].x, pv[0].y, pv[0].y);
#pragma unroll
  for (int i = 1; i < 10; ++i) { mx = max3f(mx, pv[i].x, pv[i].y); mn = min3f(mn, pv[i].x, pv[i].y); }
  const float a = -mx * C, b = mn * C;
  v2f ep[10], en[10];
#pragma unroll
  for (int i = 0; i < 10; ++i) { ep[i] = pkfma(pv[i], (v2f){C, C}, (v2f){a, a}); en[i] = pkfma(pv[i], (v2f){-C, -C}, (v2f){b, b}); }
  SB();
#pragma unroll
  for (int i = 0; i < 10; ++i) {
    ep[i].x = __builtin_amdgcn_exp2f(ep[i].x); ep[i].y = __builtin_amdgcn_exp2f(ep[i].y);
    en[i].x = __builtin_amdgcn_exp2f(en[i].x); en[i].y = __builtin_amdgcn_exp2f(en[i].y);
  }
  SB();
  v2f sp = ep[0], sn = en[0];
#pragma unroll
  for (int i = 1; i < 10; ++i) { sp += ep[i]; sn += en[i]; }
  const float rp = __builtin_amdgcn_rcpf(sp.x + sp.y), rn = __builtin_amdgcn_rcpf(sn.x + sn.y);
#pragma unroll
  for (int i = 0; i < 10; ++i) { ep[i] *= (v2f){rp, rp}; en[i] *= (v2f){rn, rn}; }
  SB();
#pragma unroll
  for (int i = 0; i < 10; ++i) { phi[i] = pk2(ep[i].x, ep[i].y); phi[10 + i] = pk2(en[i].x, en[i].y); }
}
__device__ __forceinline__ void feat_proj(const u32 (&t)[10], const v4u (&wf)[6], v2f (&pv)[10]) {
  const v4u b0 = {t[0], t[1], t[2], t[3]}, b1 = {t[4], t[5], t[6], t[7]}, b2 = {t[8], t[9], 0u, 0u};
  v16f X, Y;
  mfma32_v<true>(X, wf[0], b0); mfma32_v<true>(Y, wf[3], b0);
  mfma32_v<false>(X, wf[1], b1); mfma32_v<false>(Y, wf[4], b1);
  mfma32_v<false>(X, wf[2], b2); mfma32_v<false>(Y, wf[5], b2);
  MFMA_DONE2(X, Y);
#pragma unroll
  for (int i = 0; i < 8; ++i) pv[i] = (v2f){X[2 * i], X[2 * i + 1]};
  pv[8] = (v2f){Y[0], Y[1]}; pv[9] = (v2f){Y[2], Y[3]};
}

__global__ void __launch_bounds__(256, 1) fused_kernel(Params p) {
  extern __shared__ __attribute__((aligned(16))) char smem[];
  const lptr L0 = (lptr)smem;
  const int tid = threadIdx.x, lane = tid & 63, w = __builtin_amdgcn_readfirstlane(tid >> 6);
  const int b = blockIdx.x;
  const int tg = (b & 7) * 2 + (b >> 7), j = (b >> 3) & 15;
  const int cu = tg * 16 + j, th = j & 1, cg = j >> 1;
  const rsrc_t ws = mk_rsrc(p.ws, WS_END);
  v4i wsd; { const u64 a = (u64)p.ws; wsd.x = (int)(u32)a; wsd.y = (int)(u32)(a >> 32); wsd.z = (int)WS_END; wsd.w = 0x00020000; }
  const int tok = lane & 31, h = lane >> 5;
  const int t16 = lane & 15, g = lane >> 4;
  const u32 o_pub = WS_O + (u32)(tg * 8 + 2 * w) * 20480u + (u32)(5 * j) * 256u;
  const u32 o_lo = (u32)(tok >> 4) * 20480u + (u32)(tok & 15) * 16u;
  const u32 x_tk = (u32)((w & 1) * 16 + t16), x_lo = ((u32)(g & 1) * 32u + x_tk) * 16u;
  const u32 x_pub = WS_X + (u32)(tg * 4 + th * 2 + (w >> 1)) * 40960u + (u32)(5 * cg) * 1024u;
  STAMP(0);

  const int G = lane >> 4, q4 = (lane >> 2) & 3, p4 = lane & 3, rg = 4 * (G & 1) + p4;
  const u32 l16 = (u32)lane * 16u, w1k = (u32)w * 1024u, lw16 = l16 + w1k;
#define DMA_RUN(img, first, n) sfor<0, n>([&](auto dc) { dma<(first) + 4 * dc.value>(wsd, lw16, img, w1k); })

  const u32 epoch_v = ld4(ws, 0, 0);
  float res[20];
  v4u wf[6], fB[3];
  u32 E;
  const int ns = tg < 8 ? 3 : 2, s0 = tg < 8 ? 3 * tg : 8 + 2 * tg;
  const u32 fdst = (w == 3 ? (u32)(R2_OFF + s0 * 1024) : (u32)(s0 * R1_STEP + w * 1024)) + (w == 2 ? (u32)(h * 24 + tok) * 16u : l16);
  const u32 fstep = w == 3 ? 1024u : (u32)R1_STEP;
  const bool fon = w != 2 || tok < 24;
  {
    const rsrc_t rx = mk_rsrc(p.x0, 2048 * 640 * 2), rA = mk_rsrc(p.wqA, 640 * 1920 * 2), rB = mk_rsrc(p.wqB, 640 * 1920 * 2);
    const rsrc_t roA = mk_rsrc(p.woA, 640 * 640 * 2), roB = mk_rsrc(p.woB, 640 * 640 * 2), rp = mk_rsrc(p.wphi, 32 * 400 * 2);
    u32 src[3], stg[3];
#pragma unroll
    for (int i = 0; i < 3; ++i) {
      const int n = tid + 256 * i, row = n / 15, cc = n % 15, part = cc / 5, mem = cc % 5;
      const int T = (part + 2) % 3;
      src[i] = n < 240 * ns ? (u32)((16 * s0 + row) * 3840 + part * 1280 + j * 80 + mem * 16) : OOB;
      stg[i] = (u32)((row >> 2) * 960 + (mem == 2 ? 512 + (row & 3) * 48 + T * 16 : (T == 2 ? 704 : T * 256) + (row & 3) * 64 + (mem - (mem > 2)) * 16));
    }
    v4u ca[3], cb[3];
#pragma unroll
    for (int i = 0; i < 3; ++i) ca[i] = ld16p(rA, src[i], 0);
    const int wi = 2 * tg + th;
    v4u co[4];
#pragma unroll
    for (int i = 0; i < 2; ++i) {
      const int n = tid + 256 * i, bc = n >> 4, blk = bc / 5, c = bc % 5, i8 = (n >> 1) & 7;
      const int k = 32 * wi + 8 * (2 * (blk & 1) + (i8 >> 2)) + 4 * (blk >> 1) + (i8 & 3);
      const u32 o = (wi < 20 && n < 320) ? (u32)(k * 1280 + (80 * cg + 16 * c) * 2 + (n & 1) * 16) : OOB;
      co[2 * i] = ld16p(roA, o, 0); co[2 * i + 1] = ld16p(roB, o, 0);
    }
#pragma unroll
    for (int i = 0; i < 3; ++i) cb[i] = ld16p(rB, src[i], 0);
    v2u xr[5];
    const u32 xo = (u32)((128 * tg + 64 * th + 16 * w + t16) * 1280 + (80 * cg + 4 * g) * 2);
#pragma unroll
    for (int c = 0; c < 5; ++c) xr[c] = ld8p(rx, xo + 32 * c, 0);
    const v4u cp = ld16p(rp, tid < 100 ? (u32)(j * 1600 + tid * 16) : OOB, 0);
    SB();
    STAMP(60);
    *(LAS v4u*)(L0 + stg[0]) = ca[0]; *(LAS v4u*)(L0 + stg[1]) = ca[1];
    if (tid < 208) *(LAS v4u*)(L0 + stg[2]) = ca[2];
    *(LAS v4u*)(L0 + 23040 + tid * 16) = co[0]; *(LAS v4u*)(L0 + 28160 + tid * 16) = co[1];
    if (tid < 64) { *(LAS v4u*)(L0 + 27136 + tid * 16) = co[2]; *(LAS v4u*)(L0 + 32256 + tid * 16) = co[3]; }
    *(LAS v4u*)(L0 + 11520 + stg[0]) = cb[0]; *(LAS v4u*)(L0 + 11520 + stg[1]) = cb[1];
    if (tid < 208) *(LAS v4u*)(L0 + 11520 + stg[2]) = cb[2];
    if (tid < 100) *(LAS v4u*)(L0 + L_WPHI + tid * 16) = cp;
    if (tid < 10) *(LAS v4u*)(L0 + L_KVR + tid * 352 + 336) = (v4u){0u, 0u, 0u, 0u};
    E = __builtin_amdgcn_readfirstlane(epoch_v);
    wg_barrier();
    STAMP(61);
    {
      const lptr tb = L0 + (G >> 1) * 960 + (rg & 1) * (w == 2 ? 8 : 32) +
                      (w == 2 ? 512 + q4 * 48 + ((rg >> 1) % 3) * 16 : (w == 3 ? 704 : w * 256) + q4 * 64 + (rg >> 1) * 8);
#pragma unroll
      for (int i = 0; i < 3; ++i) {
        const v4u fa = __builtin_bit_cast(v4u, trfrag(tb + i * 3840, tb + i * 3840 + 1920));
        st16(fa, ws, (i < ns && fon) ? fdst + i * fstep : OOB, WS_WQ + (u32)j * WIMG);
      }
      if (wi < 20) {
        const lptr ob = L0 + 23040 + (G >> 1) * 1280 + ((G & 1) * 4 + q4) * 32 + p4 * 8;
        for (int f = w; f < 10; f += 4) {
          const int t2 = f >= 5, c = f - 5 * t2;
          const lptr a = ob + t2 * 5120 + c * 256;
          st16(__builtin_bit_cast(v4u, trfrag(a, a + 2560)), ws, l16, WS_WO + (u32)(t2 * 8 + cg) * 102400u + (u32)wi * 5120u + (u32)c * 1024u);
        }
      }
#pragma unroll
      for (int i = 0; i < 3; ++i) fB[i] = __builtin_bit_cast(v4u, trfrag(tb + 11520 + i * 3840, tb + 11520 + i * 3840 + 1920));
    }
    SB();
    STAMP(62);
    {
      const int t = min(tid, 239), ty = t / 120, col = t % 120, part = col / 40, c40 = col % 40, mem = c40 >> 3, T = (part + 2) % 3;
      const lptr cb0 = L0 + ty * 11520 + (c40 & 7) * 2 + (mem == 2 ? 512 + T * 16 : (T == 2 ? 704 : T * 256) + (mem - (mem > 2)) * 16);
      const int rs = mem == 2 ? 48 : 64;
      float s = 0.f;
#pragma unroll 4
      for (int kb = 0; kb < 4 * ns; ++kb) {
        const lptr a = cb0 + kb * 960;
        s += (__uint_as_float((u32) * (LAS u16*)a << 16) + __uint_as_float((u32) * (LAS u16*)(a + rs) << 16)) +
             (__uint_as_float((u32) * (LAS u16*)(a + 2 * rs) << 16) + __uint_as_float((u32) * (LAS u16*)(a + 3 * rs) << 16));
      }
      const int hh = c40 / 20, d = c40 % 20, e = hh ? (d + 16) % 20 : d;
      const int idx = hh * 64 + (e < 16 ? T * 16 + e : 48 + 4 * T + e - 16);
      st8((v2u){__float_as_uint(s), E + 1u}, ws, (u32)idx * 8u + (u32)ty * (256u * 1024u), WS_CS + (u32)(j * 16 + tg) * 1024u);
      float s1 = 0.f, s2 = 0.f;
#pragma unroll
      for (int c = 0; c < 5; ++c) {
        res[4 * c] = bflo(xr[c].x); res[4 * c + 1] = bfhi(xr[c].x); res[4 * c + 2] = bflo(xr[c].y); res[4 * c + 3] = bfhi(xr[c].y);
      }
#pragma unroll
      for (int i = 0; i < 20; ++i) { s1 += res[i]; s2 = __builtin_fmaf(res[i], res[i], s2); }
      s1 += __shfl_xor(s1, 16); s2 += __shfl_xor(s2, 16);
      s1 += __shfl_xor(s1, 32); s2 += __shfl_xor(s2, 32);
      st16((v4u){__float_as_uint(s1), E + 1u, __float_as_uint(s2), E + 1u}, ws, lane < 16 ? x_tk * 16u : OOB,
           WS_ST + (u32)((tg * 4 + th * 2 + (w >> 1)) * 8 + cg) * 512u);
    }
    SB();
    STAMP(63);
    vmwait<2>();
    wg_barrier();
    if (tid == 0) st4(E + 1u, ws, flag_off(PH_F0, cu), 0);
    STAMP(1);
    v4i xd; { const u64 a = (u64)p.x0; xd.x = (int)(u32)a; xd.y = (int)(u32)(a >> 32); xd.z = 2048 * 640 * 2; xd.w = 0x00020000; }
    const u32 xrow = (u32)(128 * tg + 32 * w) * 1280u, lw = (u32)w * 38912u;
    {
      const u32 v0 = xrow + (u32)(lane >> 2) * 1280u + (u32)((lane & 3) ^ G) * 16u, v1 = v0 + 16u * 1280u;
      SB();
      sfor<0, 19>([&](auto ic) { dma_x0<ic.value, 0>(xd, v0, lw); dma_x0<ic.value, 1>(xd, v1, lw); });
      const u32 vt = xrow + (u32)tok * 1280u + (u32)h * 8u;
      sfor<0, 4>([&](auto ic) { mem_to_acc8<AX + 152 + 2 * ic.value, 1216 + 16 * ic.value>(xd, vt); });
    }
    {
      const int r = lane & 31, m = r >> 3, hp = (r >> 2) & 1, t = r & 3;
#pragma unroll
      for (int y = 0; y < 2; ++y)
#pragma unroll
        for (int i = 0; i < 3; ++i) {
          const int f = y ? 16 + t : 4 * m + t;
          const bool ok = (hp == h) && (y == 0 || m == 0);
          u32 dw[4];
#pragma unroll
          for (int q = 0; q < 4; ++q) {
            u32 lo = 0, hi = 0;
            const int e0 = 8 * i + 2 * q;
            if (e0 < 20) {
              const int d0 = h ? (e0 + 4) % 20 : e0, d1 = h ? (e0 + 5) % 20 : e0 + 1;
              lo = *(LAS u16*)(L0 + L_WPHI + (h * 400 + d0 * 20 + f) * 2);
              hi = *(LAS u16*)(L0 + L_WPHI + (h * 400 + d1 * 20 + f) * 2);
            }
            dw[q] = ok ? (lo | (hi << 16)) : 0u;
          }
          wf[y * 3 + i] = (v4u){dw[0], dw[1], dw[2], dw[3]};
        }
      asm volatile("" : "+v"(wf[0]), "+v"(wf[1]), "+v"(wf[2]), "+v"(wf[3]), "+v"(wf[4]), "+v"(wf[5]));
    }
    STAMP(10);
    DRAIN();
    STAMP(11);
    {
      const lptr xa = L0 + lw + tok * 64 + h * 8;
      const int sw = (tok >> 2) & 3;
      const lptr c0 = xa + (0 ^ sw) * 16, c1 = xa + (1 ^ sw) * 16, c2 = xa + (2 ^ sw) * 16, c3 = xa + (3 ^ sw) * 16;
      sfor<0, 19>([&](auto ic) {
        constexpr int k = ic.value;
        lds_to_acc8<AX + 8 * k, k * 2048>(c0); lds_to_acc8<AX + 8 * k + 2, k * 2048>(c1);
        lds_to_acc8<AX + 8 * k + 4, k * 2048>(c2); lds_to_acc8<AX + 8 * k + 6, k * 2048>(c3);
      });
    }
    wg_barrier();
  }
  STAMP(2);

#pragma unroll 1
  for (int L = 0; L < 4; ++L) {
    const int ty = L & 1;
    const u32 tagL = E + 1u + (u32)L;
    const u32 imgW = WS_WQ + (u32)(ty * 16 + j) * WIMG, imgN = WS_WQ + (u32)((ty ^ 1) * 16 + j) * WIMG;
    const u32 imgO = WS_WO + (u32)(ty * 8 + cg) * 102400u - (u32)L_WO;
    const u32 xb = WS_X + (u32)((L * 16 + tg) * 4 + w) * 40960u;
    if (L == 0) {
      poll_flags(ws, flag_off(PH_F0, lane < 16 ? lane * 16 + j : ((lane - 16) >> 1) * 16 + 2 * cg + (lane & 1)), lane < 36, E + 1u);
      STAMP(3);
      const u32 cso = WS_CS + (u32)(((tid >> 7) * 16 + j) * 16) * 1024u + (u32)(tid & 127) * 8u;
      const bool pad = (tid & 63) >= 60;
      DMA_RUN(imgW, 0, 9);
      v2u csg[16];
      for (u32 sp = 0; sp < SPIN_CAP; ++sp) {
        asm volatile("" ::: "memory");
#pragma unroll
        for (int i = 0; i < 16; ++i) csg[i] = ld8(ws, cso + i * 1024u, 0);
        SB();
        bool ok = true;
#pragma unroll
        for (int i = 0; i < 16; ++i) ok &= csg[i].y == E + 1u;
        if (__all(ok || pad)) break;
      }
      float s = 0.f;
#pragma unroll
      for (int i = 0; i < 16; ++i) s += __uint_as_float(csg[i].x);
      *(LAS float*)(L0 + L_CS + tid * 4) = pad ? 0.f : s;
    } else {
      const u32 po = lane < 16 ? xb + (u32)(5 * (lane >> 1) + 4) * 1024u + (u32)((lane & 1) * 16 + 15) * 16u + 8u : OOB;
      lptr pk0 = L0 + 36864 + lw16, pk1 = pk0 + 40960;
      asm volatile("" : "+v"(pk0), "+v"(pk1));
      sfor<0, 19>([&](auto ic) { park_store<ic.value>(ic.value < 10 ? pk0 : pk1); });
      for (u32 sp = 0; sp < SPIN_CAP; ++sp) {
        u32 v;
        asm volatile("buffer_load_dword %0, %1, %2, 0 offen sc1" : "=v"(v) : "v"(po), "s"(wsd));
        sfor<0, 4>([&](auto ic) { ld_frag<ic.value>(wsd, l16, xb); });
        asm volatile("s_waitcnt vmcnt(4)" : "+v"(v));
        if (__all(v != SENT)) break;
      }
    }
    STAMP(4 + L * 14 + 0);
    const u32 sto = WS_ST + (u32)(((L * 16 + tg) * 4 + w) * 8 + 4 * h) * 512u + (u32)tok * 16u;
    v16f aK, aV, aT, aQ;
    v4u st[4];
    auto pass1 = [&](auto fc) {
      lptr m0 = L0 + l16, m1 = m0 + 20 * R1_STEP, t0 = L0 + 2048 + (h * 24 + tok) * 16, t1 = t0 + 20 * R1_STEP;
      asm volatile("" : "+v"(m0), "+v"(m1), "+v"(t0), "+v"(t1));
      v4u f0 = *(LAS v4u*)m0, f1 = *(LAS v4u*)(m0 + 1024), f2 = *(LAS v4u*)t0;
      const v4u sv = {SENT, SENT, SENT, SENT};
      const u32 on = o_pub + (u32)(L + 1) * (128u * 20480u), xn = x_pub + (u32)(L + 1) * (64u * 40960u);
      SB();
      sfor<0, 40>([&](auto ic) {
        constexpr int s = ic.value, n = s + 1;
        constexpr bool FIRST = decltype(fc)::value;
        const lptr mb = (n < 20 ? m0 : m1) + (n % 20) * R1_STEP, tb = (n < 20 ? t0 : t1) + (n % 20) * R1_STEP;
        if constexpr (s == 32) st16(sv, ws, o_lo + (u32)h * 768u, on);
        if constexpr (s == 33) st16(sv, ws, o_lo + (u32)h * 768u + 256u, on);
        if constexpr (s == 34) st8((v2u){SENT, SENT}, ws, o_lo + 512u + (u32)h * 8u, on);
        if constexpr (s == 35) st16(sv, ws, x_lo + (u32)h * 1024u, xn);
        if constexpr (s == 36) st16(sv, ws, x_lo + (u32)h * 1024u + 2048u, xn);
        if constexpr (s == 37) st8((v2u){SENT, SENT}, ws, x_lo + 4096u + (u32)h * 8u, xn);
        SB();
        if constexpr (!FIRST) {
          if constexpr (s < 32) ld_frag<s + 8>(wsd, l16, xb);
          vmwait<p1_younger(s) + p1_first(s)>();
        } else {
          if constexpr (s == 12 || s == 19 || s == 29) {
            vmwait<(s == 12 ? 7 : s == 19 ? 11 : 17)>();
            asm volatile("s_barrier" ::: "memory");
          }
          if constexpr (s < 19) dma<36 + 4 * s>(wsd, lw16, imgW, w1k);
          else if constexpr (s < 22) dma<136 + 4 * (s - 19)>(wsd, lw16, imgW, w1k);
          else if constexpr (s == 22) dma<146>(wsd, lw16, imgW, w1k);
          else if constexpr (s < 26) st16(fB[s - 23], ws, (s - 23 < ns && fon) ? fdst + (s - 23) * fstep : OOB, imgN);
          else if constexpr (s == 26) { st16(sv, ws, o_lo + (u32)h * 768u, o_pub); st16(sv, ws, o_lo + (u32)h * 768u + 256u, o_pub); }
          else if constexpr (s == 27) st8((v2u){SENT, SENT}, ws, o_lo + 512u + (u32)h * 8u, o_pub);
          else if constexpr (s < 30) st16(sv, ws, tid * 16u, WS_HK + (u32)((s - 28) * 256 + cu) * 4096u);
          else if constexpr (s < 32) st16(sv, ws, tid < 224 ? tid * 16u : OOB, WS_KV + (u32)(((s - 30) * 16 + j) * 16 + tg) * 3584u);
        }
        SB();
        mfma32_x<s, s == 0>(aK, asbf(f0));
        SB();
        if constexpr (n < 40) f0 = *(LAS v4u*)mb;
        SB();
        mfma32_x<s, s == 0>(aV, asbf(f1));
        if constexpr (s >= 12 && s <= 22 && !(s & 1)) dma<112 + 2 * (s - 12)>(wsd, lw16, imgW, w1k);
        if constexpr (s >= 24 && s < 28)
          asm volatile("buffer_load_dwordx4 %0, %1, %2, 0 offen offset:%c3 sc1" : "=v"(st[s - 24]) : "v"(sto), "s"(wsd), "i"((s - 24) * 512));
        SB();
        if constexpr (n < 40) f1 = *(LAS v4u*)(mb + 1024);
        SB();
        mfma32_x<s, s == 0>(aT, asbf(f2));
        SB();
        if constexpr (n < 40) f2 = *(LAS v4u*)tb;
        SB();
      });
      asm volatile("s_waitcnt vmcnt(6)\n\ts_nop 10" : "+v"(aK), "+v"(aV), "+v"(aT), "+v"(st[0]), "+v"(st[1]), "+v"(st[2]), "+v"(st[3]));
    };
    if (L == 0) {
      wg_barrier();
      STAMP(4 + L * 14 + 1);
      pass1(IC<1>{});
    } else {
#pragma unroll 1
      for (int att = 0; att < 2; ++att) {
        SB();
        if (att == 1) sfor<0, 4>([&](auto ic) { ld_frag<ic.value>(wsd, l16, xb); });
        sfor<4, 8>([&](auto ic) { ld_frag<ic.value>(wsd, l16, xb); });
        if (att == 0) wg_barrier();
        STAMP(4 + L * 14 + 1);
        pass1(IC<0>{});
        if (att == 1 || !__any(aK[0] != aK[0])) break;
        strict_wait(ws, l16, xb, 40);
      }
    }
    STAMP(4 + L * 14 + 2);
    {
      bool ok = true;
#pragma unroll
      for (int i = 0; i < 4; ++i) ok &= (st[i].y == tagL) & (st[i].w == tagL);
      if (!__all(ok)) {
        for (u32 sp = 0; sp < SPIN_CAP; ++sp) {
          asm volatile("" ::: "memory");
#pragma unroll
          for (int i = 0; i < 4; ++i) st[i] = ld16(ws, sto + i * 512u, 0);
          ok = true;
#pragma unroll
          for (int i = 0; i < 4; ++i) ok &= (st[i].y == tagL) & (st[i].w == tagL);
          if (__all(ok)) break;
        }
      }
    }
    const LAS v4f* cs = (const LAS v4f*)(L0 + L_CS + (ty * 2 + h) * 256);
    v4f cc[11];
#pragma unroll
    for (int i = 0; i < 11; ++i) cc[i] = cs[i < 8 ? i : i + 4];
    v2f nmean, rstd;
    {
      float s1 = (__uint_as_float(st[0].x) + __uint_as_float(st[1].x)) + (__uint_as_float(st[2].x) + __uint_as_float(st[3].x));
      float s2 = (__uint_as_float(st[0].z) + __uint_as_float(st[1].z)) + (__uint_as_float(st[2].z) + __uint_as_float(st[3].z));
      auto r1 = __builtin_amdgcn_permlane32_swap(__float_as_uint(s1), __float_as_uint(s1), false, false);
      auto r2 = __builtin_amdgcn_permlane32_swap(__float_as_uint(s2), __float_as_uint(s2), false, false);
      s1 = __uint_as_float(r1[0]) + __uint_as_float(r1[1]);
      s2 = __uint_as_float(r2[0]) + __uint_as_float(r2[1]);
      const float mean = s1 * (1.f / 640.f);
      const float r = __builtin_amdgcn_rsqf(fmaxf(s2 * (1.f / 640.f) - mean * mean, 0.f) + 1e-5f);
      nmean = (v2f){-mean, -mean}; rstd = (v2f){r, r};
    }
    asm volatile("" : "+v"(cc[0]), "+v"(cc[1]), "+v"(cc[2]), "+v"(cc[3]), "+v"(cc[4]), "+v"(cc[5]), "+v"(cc[6]), "+v"(cc[7]),
                 "+v"(cc[8]), "+v"(cc[9]), "+v"(cc[10]));
#define LN2(acc, i, c4) (pkfma(nmean, (i & 1) ? (v2f){(c4).z, (c4).w} : (v2f){(c4).x, (c4).y}, (v2f){(acc)[2 * (i)], (acc)[2 * (i) + 1]}))
    v2f kk[10], vv[10], qt[2];
#pragma unroll
    for (int i = 0; i < 8; ++i) { kk[i] = LN2(aK, i, cc[i >> 1]); vv[i] = LN2(aV, i, cc[4 + (i >> 1)]); }
#pragma unroll
    for (int i = 0; i < 2; ++i) { kk[8 + i] = LN2(aT, i, cc[8]); vv[8 + i] = LN2(aT, 2 + i, cc[9]); qt[i] = LN2(aT, 4 + i, cc[10]); }
#pragma unroll
    for (int i = 0; i < 10; ++i) { kk[i] *= rstd; vv[i] *= rstd; }
    SB();
    u32 kp[10];
#pragma unroll
    for (int i = 0; i < 10; ++i) kp[i] = pk2(kk[i].x, kk[i].y);
    if (ty == 0) {
      if (w == 3 && tg < 15 && tok >= 16) {
        const u32 hb = WS_HK + (u32)((L >> 1) * 256 + cu) * 4096u, ho = (u32)(h * 16 + tok - 16) * 16u;
#pragma unroll
        for (int i = 0; i < 5; ++i)
          st16(__builtin_bit_cast(v4u, __builtin_shufflevector(vv[2 * i], vv[2 * i + 1], 0, 1, 2, 3)), ws, ho + i * 512u, hb);
        st16((v4u){kp[0], kp[1], kp[2], kp[3]}, ws, ho + 2560u, hb);
        st16((v4u){kp[4], kp[5], kp[6], kp[7]}, ws, ho + 3072u, hb);
        st16((v4u){kp[8], kp[9], 0u, 0u}, ws, ho + 3584u, hb);
      }
      wg_barrier();
      if (L == 0 && tid == 0) st4(E + 1u, ws, flag_off(PH_F1, cu), 0);
      const int row = h * 144 + 16 + 32 * w + tok;
      LAS v4f* vp = (LAS v4f*)(L0 + L_VP + row * 80);
      LAS v4u* kq = (LAS v4u*)(L0 + L_KP + row * 48);
#pragma unroll
      for (int i = 0; i < 5; ++i) vp[i] = __builtin_shufflevector(vv[2 * i], vv[2 * i + 1], 0, 1, 2, 3);
      kq[0] = (v4u){kp[0], kp[1], kp[2], kp[3]}; kq[1] = (v4u){kp[4], kp[5], kp[6], kp[7]}; *(LAS v2u*)(kq + 2) = (v2u){kp[8], kp[9]};
    } else {
      v2f pv[10];
      u32 phi[20], vq[10];
      feat_proj(kp, wf, pv);
#pragma unroll
      for (int i = 0; i < 10; ++i) vq[i] = pk2(vv[i].x, vv[i].y);
      dual_softmax(pv, phi);
      wg_barrier();
      const int row = h * 128 + 32 * w + tok, sw = (tok >> 1) & 3;
      const lptr pa = L0 + L_PA + row * 64, pb = L0 + L_PB + row * 64;
#pragma unroll
      for (int i = 0; i < 4; ++i) *(LAS v4u*)(pa + ((i ^ sw) * 16)) = (v4u){phi[4 * i], phi[4 * i + 1], phi[4 * i + 2], phi[4 * i + 3]};
      *(LAS v4u*)(pb + ((0 ^ sw) * 16)) = (v4u){vq[0], vq[1], vq[2], vq[3]};
      *(LAS v4u*)(pb + ((1 ^ sw) * 16)) = (v4u){vq[4], vq[5], vq[6], vq[7]};
      *(LAS v4u*)(pb + ((2 ^ sw) * 16)) = (v4u){vq[8], vq[9], 0x3f80u, 0u};
      *(LAS v4u*)(pb + ((3 ^ sw) * 16)) = (v4u){phi[16], phi[17], phi[18], phi[19]};
      wg_barrier();
      const int hd = w >> 1, part = w & 1, mm = rg >> 1, hh = rg & 1;
      const int trow = (hd * 128 + 8 * (G >> 1) + q4) * 64, s0 = q4 >> 1, s1 = (2 + s0) & 3;
      const int ca = (2 * hh + (mm >> 1)) ^ (part ? 3 : 0);
      const lptr a0 = L0 + (part ? L_PB : L_PA) + trow + ((ca ^ s0) * 16) + (mm & 1) * 8;
      const lptr a1 = L0 + (part ? L_PB : L_PA) + trow + 256 + ((ca ^ s1) * 16) + (mm & 1) * 8;
      const lptr b0 = L0 + L_PB + trow + (((rg >> 1) ^ s0) * 16) + (rg & 1) * 8;
      const lptr b1 = L0 + L_PB + trow + 256 + (((rg >> 1) ^ s1) * 16) + (rg & 1) * 8;
      v8bf fa[8], fb[8];
#pragma unroll
      for (int s = 0; s < 8; ++s) { fa[s] = trfrag(a0 + s * 1024, a1 + s * 1024); fb[s] = trfrag(b0 + s * 1024, b1 + s * 1024); }
      SB();
      v16f kv;
      sfor<0, 8>([&](auto ic) { constexpr int s = ic.value; mfma32_v<s == 0>(kv, fa[s], fb[s]); });
      MFMA_DONE1(kv);
      const u32 kb = WS_KV + (u32)(((L >> 1) * 16 + j) * 16 + tg) * 3584u;
      if (tok < 21 && (part == 0 || h == 0)) {
        const u32 o0 = (u32)((hd * 5 + (part ? 4 : 2 * h)) * 21 + tok) * 16u;
        st16((v4u){pk2(kv[0], kv[1]), pk2(kv[2], kv[3]), pk2(kv[4], kv[5]), pk2(kv[6], kv[7])}, ws, o0, kb);
        if (part == 0) st16((v4u){pk2(kv[8], kv[9]), pk2(kv[10], kv[11]), pk2(kv[12], kv[13]), pk2(kv[14], kv[15])}, ws, o0 + 336u, kb);
      }
    }
    STAMP(4 + L * 14 + 3);
    {
      lptr bq = L0 + R2_OFF + l16;
      asm volatile("" : "+v"(bq));
      v4u fq[4];
#pragma unroll
      for (int i = 0; i < 4; ++i) fq[i] = *(LAS v4u*)(bq + i * 1024);
      SB();
      sfor<0, 40>([&](auto ic) {
        constexpr int s = ic.value;
        if constexpr (!(s & 1)) asm volatile("" : "+v"(fq[s & 3]), "+v"(fq[(s + 1) & 3]));
        mfma32_x<s, s == 0>(aQ, asbf(fq[s & 3]));
        if constexpr (s % 3 == 0 && s < 36) dma<36 + 4 * (s / 3)>(wsd, lw16, imgO, w1k);
        SB();
        if constexpr (s + 4 < 40) fq[s & 3] = *(LAS v4u*)(bq + (s + 4) * 1024);
        SB();
      });
    }
    if (L == 0) poll_flags(ws, flag_off(PH_F1, lane < 16 ? tg * 16 + lane : (lane & 15) * 16 + j), lane < 32, E + 1u);
    auto make_q = [&](float qs, u32 (&qp)[10]) {
      v4f cq[4];
#pragma unroll
      for (int i = 0; i < 4; ++i) cq[i] = cs[8 + i];
      SB();
      asm volatile("s_nop 11" : "+v"(aQ), "+v"(cq[0]), "+v"(cq[1]), "+v"(cq[2]), "+v"(cq[3]));
      v2f qq[10];
#pragma unroll
      for (int i = 0; i < 8; ++i) qq[i] = LN2(aQ, i, cq[i >> 1]) * (v2f){qs, qs};
      qq[8] = qt[0] * (v2f){qs, qs}; qq[9] = qt[1] * (v2f){qs, qs};
      SB();
#pragma unroll
      for (int i = 0; i < 10; ++i) qp[i] = pk2(qq[i].x, qq[i].y);
    };
    STAMP(4 + L * 14 + 4);
    v2f oo[10];
    if (ty == 0) {
      u32 qp[10];
      if (w == 0) {
        const u32 hkb = WS_HK + (u32)((L >> 1) * 256 + cu - 16) * 4096u;
        v4u pr[4];
#pragma unroll
        for (int i = 0; i < 4; ++i) pr[i] = (v4u){0u, 0u, 0u, 0u};
        if (tg > 0) {
          for (u32 sp = 0; sp < SPIN_CAP; ++sp) {
            asm volatile("" ::: "memory");
#pragma unroll
            for (int i = 0; i < 4; ++i) pr[i] = ld16(ws, l16 + i * 1024u, hkb);
            if (__all(max(max(max4(pr[0]), max4(pr[1])), max(max4(pr[2]), max4(pr[3]))) != SENT)) break;
          }
        }
        const int row = ((lane >> 4) & 1) * 144 + (lane & 15);
#pragma unroll
        for (int i = 0; i < 4; ++i) {
          const int c = 2 * i + h;
          *(LAS v4u*)(L0 + (c < 5 ? L_VP + row * 80 + c * 16 : L_KP + row * 48 + (c - 5) * 16)) = pr[i];
        }
      }
      make_q(rstd.x * (0.22360679774997896f * 1.4426950408889634f), qp);
      vmwait<12>();
      wg_barrier();
      STAMP(4 + L * 14 + 5);
      const int row = h * 144 + 2 + 32 * w + tok;
      lptr kr = L0 + L_KP + row * 48, vr = L0 + L_VP + row * 80;
      asm volatile("" : "+v"(kr), "+v"(vr));
      v4u k0[8], k1[8];
      v2u k2[8];
      v4f va[3][5];
      float sc[8];
#pragma unroll
      for (int i = 0; i < 4; ++i) {
        k0[i] = *(LAS v4u*)(kr + (7 - i) * 96); k1[i] = *(LAS v4u*)(kr + (7 - i) * 96 + 16); k2[i] = *(LAS v2u*)(kr + (7 - i) * 96 + 32);
      }
      SB();
      sfor<0, 8>([&](auto ic) {
        constexpr int i = ic.value;
        asm volatile("" : "+v"(k0[i]), "+v"(k1[i]), "+v"(k2[i]));
        if constexpr (i < 4) {
          k0[i + 4] = *(LAS v4u*)(kr + (3 - i) * 96); k1[i + 4] = *(LAS v4u*)(kr + (3 - i) * 96 + 16); k2[i + 4] = *(LAS v2u*)(kr + (3 - i) * 96 + 32);
        } else if constexpr (i >= 6) {
#pragma unroll
          for (int c = 0; c < 5; ++c) va[i - 6][c] = *(LAS v4f*)(vr + (13 - i) * 160 + c * 16);
        }
        SB();
        if constexpr (i < 7) dma<84 + 8 * i>(wsd, lw16, imgO, w1k);
        if constexpr (i < 6) dma<88 + 8 * i>(wsd, lw16, imgO, w1k);
        float a = dot2(qp[0], k0[i].x, 0.f);
        a = dot2(qp[1], k0[i].y, a); a = dot2(qp[2], k0[i].z, a); a = dot2(qp[3], k0[i].w, a);
        a = dot2(qp[4], k1[i].x, a); a = dot2(qp[5], k1[i].y, a); a = dot2(qp[6], k1[i].z, a); a = dot2(qp[7], k1[i].w, a);
        a = dot2(qp[8], k2[i].x, a); a = dot2(qp[9], k2[i].y, a);
        sc[i] = a;
        SB();
      });
      if (tg == 0 && w == 0) {
#pragma unroll
        for (int i = 1; i < 8; ++i) if (tok - 2 * i < 0) sc[i] = -1e9f * 1.4426950408889634f;
      }
      const float mx = fmaxf(fmaxf(fmaxf(fmaxf(sc[0], sc[1]), sc[2]), fmaxf(fmaxf(sc[3], sc[4]), sc[5])), fmaxf(sc[6], sc[7]));
#pragma unroll
      for (int i = 0; i < 8; ++i) sc[i] = __builtin_amdgcn_exp2f(sc[i] - mx);
      const float rs = __builtin_amdgcn_rcpf(((sc[0] + sc[1]) + (sc[2] + sc[3])) + ((sc[4] + sc[5]) + (sc[6] + sc[7])));
      SB();
      sfor<0, 8>([&](auto ic) {
        constexpr int i = ic.value;
        asm volatile("" : "+v"(va[i % 3][0]), "+v"(va[i % 3][1]), "+v"(va[i % 3][2]), "+v"(va[i % 3][3]), "+v"(va[i % 3][4]));
        if constexpr (i + 2 < 8) {
#pragma unroll
          for (int c = 0; c < 5; ++c) va[(i + 2) % 3][c] = *(LAS v4f*)(vr + (5 - i) * 160 + c * 16);
        }
        SB();
        if constexpr (i < 6) park_load<i>(wsd, lw16, imgN);
        const v2f pp = {sc[i], sc[i]};
#pragma unroll
        for (int c = 0; c < 5; ++c) {
          if constexpr (i == 0) { oo[2 * c] = pp * va[0][c].xy; oo[2 * c + 1] = pp * va[0][c].zw; }
          else { oo[2 * c] = pkfma(pp, va[i % 3][c].xy, oo[2 * c]); oo[2 * c + 1] = pkfma(pp, va[i % 3][c].zw, oo[2 * c + 1]); }
        }
        SB();
      });
#pragma unroll
      for (int e = 0; e < 10; ++e) oo[e] *= (v2f){rs, rs};
    } else {
      const u32 kvb = WS_KV + (u32)(((L >> 1) * 16 + j) * 16) * 3584u, kvo = (u32)min(tid, 209) * 16u;
      v4u pr[16];
#pragma unroll
      for (int i = 0; i < 16; ++i) ld16_asm(pr[i], wsd, kvo + i * 3584u, kvb);
      SB();
      u32 qp[10];
      make_q(rstd.x, qp);
      DMA_RUN(imgO, 84, 3);
      v2f pv[10];
      u32 phi[20];
      feat_proj(qp, wf, pv);
      DMA_RUN(imgO, 96, 3);
      dual_softmax(pv, phi);
      asm volatile("" : "+v"(phi[0]), "+v"(phi[1]), "+v"(phi[2]), "+v"(phi[3]), "+v"(phi[4]), "+v"(phi[5]), "+v"(phi[6]), "+v"(phi[7]), "+v"(phi[8]), "+v"(phi[9]),
                   "+v"(phi[10]), "+v"(phi[11]), "+v"(phi[12]), "+v"(phi[13]), "+v"(phi[14]), "+v"(phi[15]), "+v"(phi[16]), "+v"(phi[17]), "+v"(phi[18]), "+v"(phi[19]));
      SB();
      sfor<0, 6>([&](auto ic) { park_load<ic.value>(wsd, lw16, imgN); });
      STAMP(4 + L * 14 + 5);
      asm volatile("s_waitcnt vmcnt(12)" : "+v"(pr[0]), "+v"(pr[1]), "+v"(pr[2]), "+v"(pr[3]), "+v"(pr[4]), "+v"(pr[5]), "+v"(pr[6]), "+v"(pr[7]),
                   "+v"(pr[8]), "+v"(pr[9]), "+v"(pr[10]), "+v"(pr[11]), "+v"(pr[12]), "+v"(pr[13]), "+v"(pr[14]), "+v"(pr[15]));
      STAMP(4 + L * 14 + 6);
      {
        float a[8];
        u32 sel0 = 0x00003f80u, sel1 = 0x3f800000u;
        asm volatile("" : "+s"(sel0), "+s"(sel1));
#pragma unroll
        for (int c = 0; c < 8; ++c) a[c] = 0.f;
#pragma unroll
        for (int i = 0; i < 16; ++i) {
#pragma unroll
          for (int c = 0; c < 4; ++c) { a[2 * c] = dot2(sel0, pr[i][c], a[2 * c]); a[2 * c + 1] = dot2(sel1, pr[i][c], a[2 * c + 1]); }
          SB();
        }
        const float t = (a[0] + a[2]) + (a[4] + a[6]);
        if (__any(t != t)) {
#pragma unroll
          for (int c = 0; c < 8; ++c) a[c] = 0.f;
          v4u q[16];
          for (u32 sp = 0; sp < SPIN_CAP; ++sp) {
            asm volatile("" ::: "memory");
#pragma unroll
            for (int i = 0; i < 16; ++i) q[i] = ld16(ws, kvo, kvb + i * 3584u);
            SB();
            u32 mxs = 0;
#pragma unroll
            for (int i = 0; i < 16; ++i) mxs = max(mxs, max4(q[i]));
            if (__all(mxs != SENT)) break;
          }
#pragma unroll
          for (int i = 0; i < 16; ++i)
#pragma unroll
            for (int c = 0; c < 4; ++c) { a[2 * c] = dot2(sel0, q[i][c], a[2 * c]); a[2 * c + 1] = dot2(sel1, q[i][c], a[2 * c + 1]); }
        }
        if (tid < 210)
          *(LAS v4u*)(L0 + L_KVR + (tid / 21) * 352 + (tid % 21) * 16) = (v4u){pk2(a[0], a[1]), pk2(a[2], a[3]), pk2(a[4], a[5]), pk2(a[6], a[7])};
      }
      wg_barrier();
      STAMP(4 + L * 14 + 7);
      const int m = tok >> 3, hp = (tok >> 2) & 1, t = tok & 3;
      const int ex = hp == h ? 4 * m + t : 21;
      const int ey = (hp == h && (m == 0 || (m == 1 && t == 0))) ? 16 + 4 * m + t : 21;
      const lptr kx = L0 + L_KVR + h * 1760 + ex * 16, ky = L0 + L_KVR + h * 1760 + ey * 16;
      v4u ax[5], ay[5];
#pragma unroll
      for (int i = 0; i < 5; ++i) { ay[i] = *(LAS v4u*)(ky + i * 352); ax[i] = *(LAS v4u*)(kx + i * 352); }
      SB();
      v16f X, Y;
      sfor<0, 5>([&](auto ic) {
        constexpr int i = ic.value;
        const v4u bfr = {phi[4 * i], phi[4 * i + 1], phi[4 * i + 2], phi[4 * i + 3]};
        mfma32_v<i == 0>(Y, ay[i], bfr);
        mfma32_v<i == 0>(X, ax[i], bfr);
      });
      MFMA_DONE2(X, Y);
      const float rd = __builtin_amdgcn_rcpf(Y[4] + 1e-6f);
#pragma unroll
      for (int i = 0; i < 8; ++i) oo[i] = (v2f){X[2 * i], X[2 * i + 1]} * (v2f){rd, rd};
      oo[8] = (v2f){Y[0], Y[1]} * (v2f){rd, rd}; oo[9] = (v2f){Y[2], Y[3]} * (v2f){rd, rd};
    }
    SB();
    {
      const u32 ob = o_pub + (u32)L * (128u * 20480u);
      st16((v4u){pk2(oo[0].x, oo[0].y), pk2(oo[1].x, oo[1].y), pk2(oo[2].x, oo[2].y), pk2(oo[3].x, oo[3].y)}, ws, o_lo + (u32)h * 768u, ob);
      st16((v4u){pk2(oo[4].x, oo[4].y), pk2(oo[5].x, oo[5].y), pk2(oo[6].x, oo[6].y), pk2(oo[7].x, oo[7].y)}, ws, o_lo + (u32)h * 768u + 256u, ob);
      st8((v2u){pk2(oo[8].x, oo[8].y), pk2(oo[9].x, oo[9].y)}, ws, o_lo + 512u + (u32)h * 8u, ob);
    }
    SB();
    STAMP(4 + L * 14 + 8);
    if (ty == 1) DMA_RUN(imgO, 108, 7);
    if (L < 3) sfor<6, 19>([&](auto ic) { park_load<ic.value>(wsd, lw16, imgN); });
    STAMP(4 + L * 14 + 9);
    const u32 ob = WS_O + (u32)((L * 16 + tg) * 8 + th * 4 + w) * 20480u;
    {
      const u32 po = lane < 16 ? ob + (u32)(5 * lane + 2) * 256u + 248u : OOB;
      for (u32 sp = 0; sp < SPIN_CAP; ++sp) {
        u32 v;
        asm volatile("buffer_load_dword %0, %1, %2, 0 offen sc1" : "=v"(v) : "v"(po), "s"(wsd));
        sfor<0, 4>([&](auto ic) { ld_frag<ic.value>(wsd, l16, ob); });
        asm volatile("s_waitcnt vmcnt(4)" : "+v"(v));
        if (__all(v != SENT)) break;
      }
    }
    STAMP(4 + L * 14 + 10);
    v4f ac[5];
#pragma unroll 1
    for (int att = 0; att < 2; ++att) {
      SB();
      if (att == 1) sfor<0, 4>([&](auto ic) { ld_frag<ic.value>(wsd, l16, ob); });
      sfor<4, 8>([&](auto ic) { ld_frag<ic.value>(wsd, l16, ob); });
      if (att == 0) asm volatile("s_barrier" ::: "memory");
      lptr w0 = L0 + L_WO + l16, w1 = w0 + 10 * 5120;
      asm volatile("" : "+v"(w0), "+v"(w1));
      v4u fa[3][5];
#pragma unroll
      for (int c = 0; c < 5; ++c) fa[0][c] = *(LAS v4u*)(w0 + c * 1024);
#pragma unroll
      for (int c = 0; c < 5; ++c) fa[1][c] = *(LAS v4u*)(w0 + 5120 + c * 1024);
      SB();
      sfor<0, 20>([&](auto ic) {
        constexpr int s = ic.value, n = s + 2;
        const lptr wb = (n < 10 ? w0 : w1) + (n % 10) * 5120;
        asm volatile("s_waitcnt vmcnt(%c5)" : "+v"(fa[s % 3][0]), "+v"(fa[s % 3][1]), "+v"(fa[s % 3][2]), "+v"(fa[s % 3][3]), "+v"(fa[s % 3][4]) : "i"(pp_younger(s)));
        sfor<0, 5>([&](auto jc) {
          constexpr int c = jc.value;
          mfma16_x<s, s == 0>(ac[c], asbf(fa[s % 3][c]));
          if constexpr (c == 0 && s < 12) ld_frag<s + 8>(wsd, l16, ob);
          if constexpr ((c == 0 || c == 3) && s >= 12 && s < 18) {
            constexpr int q = 2 * (s - 12) + (c == 3);
            dma<(q < 9 ? 4 * q : 136 + 4 * (q - 9))>(wsd, lw16, imgN, w1k);
          }
          if constexpr (c == 0 && s == 18) dma<146>(wsd, lw16, imgN, w1k);
          SB();
          if constexpr (n < 20) fa[n % 3][c] = *(LAS v4u*)(wb + c * 1024);
          SB();
        });
      });
      asm volatile("s_nop 7" : "+v"(ac[0]), "+v"(ac[1]), "+v"(ac[2]), "+v"(ac[3]), "+v"(ac[4]));
      if (att == 1 || !__any(ac[0][0] != ac[0][0])) break;
      strict_wait(ws, l16, ob, 20);
    }
    STAMP(4 + L * 14 + 11);
    v2u xr[5];
#pragma unroll
    for (int c = 0; c < 5; ++c) {
      xr[c].x = pk2(res[4 * c] + ac[c][0], res[4 * c + 1] + ac[c][1]);
      xr[c].y = pk2(res[4 * c + 2] + ac[c][2], res[4 * c + 3] + ac[c][3]);
    }
    if (L == 3) {
      GAS char* op = (GAS char*)p.out + ((128 * tg + 64 * th + 16 * w + t16) * 1280 + (80 * cg + 4 * g) * 2);
#pragma unroll
      for (int c = 0; c < 5; ++c) *(GAS v2u*)(op + 32 * c) = xr[c];
      if (b == 0 && tid == 0) st4(E + 16u, ws, 0, 0);
    } else {
      const u32 xn = x_pub + (u32)(L + 1) * (64u * 40960u);
#pragma unroll
      for (int c = 0; c < 4; c += 2) {
        auto r0 = __builtin_amdgcn_permlane32_swap(xr[c].x, xr[c + 1].x, false, false);
        auto r1 = __builtin_amdgcn_permlane32_swap(xr[c].y, xr[c + 1].y, false, false);
        st16((v4u){r0[0], r1[0], r0[1], r1[1]}, ws, x_lo + (u32)(c + h) * 1024u, xn);
      }
      st8(xr[4], ws, x_lo + 4096u + (u32)h * 8u, xn);
      SB();
      STAMP(4 + L * 14 + 12);
      float s1 = 0.f, s2 = 0.f;
#pragma unroll
      for (int c = 0; c < 5; ++c) {
        res[4 * c] = bflo(xr[c].x); res[4 * c + 1] = bfhi(xr[c].x); res[4 * c + 2] = bflo(xr[c].y); res[4 * c + 3] = bfhi(xr[c].y);
      }
#pragma unroll
      for (int i = 0; i < 20; ++i) { s1 += res[i]; s2 = __builtin_fmaf(res[i], res[i], s2); }
      s1 += __shfl_xor(s1, 16); s2 += __shfl_xor(s2, 16);
      s1 += __shfl_xor(s1, 32); s2 += __shfl_xor(s2, 32);
      st16((v4u){__float_as_uint(s1), tagL + 1u, __float_as_uint(s2), tagL + 1u}, ws, lane < 16 ? x_tk * 16u : OOB,
           WS_ST + (u32)((((L + 1) * 16 + tg) * 4 + th * 2 + (w >> 1)) * 8 + cg) * 512u);
      SB();
      asm volatile("s_barrier" ::: "memory");
    }
    STAMP(4 + L * 14 + 13);
  }
  DRAIN();
}

extern "C" void kernel_launch(void* const* d_in, const int* in_sizes, int n_in, void* d_out, int out_size, void* d_ws,
                              size_t ws_size, hipStream_t stream) {
  Params p;
  p.x0 = (const u16*)d_in[0]; p.wqA = (const u16*)d_in[1]; p.woA = (const u16*)d_in[2];
  p.wqB = (const u16*)d_in[4]; p.wphi = (const u16*)d_in[5]; p.woB = (const u16*)d_in[6];
  p.out = (u16*)d_out; p.ws = (unsigned char*)d_ws;
  (void)hipFuncSetAttribute((const void*)fused_kernel, hipFuncAttributeMaxDynamicSharedMemorySize, LDS_BYTES);
  fused_kernel<<<256, 256, LDS_BYTES, stream>>>(p);
}
```

```cpp
#include <hip/hip_runtime.h>
#include <hip/hip_bf16.h>

typedef unsigned short u16;
typedef unsigned u32;
typedef unsigned long long u64;
typedef u32 v4u __attribute__((ext_vector_type(4)));
typedef u32 v2u __attribute__((ext_vector_type(2)));
typedef int v4i __attribute__((ext_vector_type(4)));
typedef float v4f __attribute__((ext_vector_type(4)));
typedef float v2f __attribute__((ext_vector_type(2)));
typedef float v16f __attribute__((ext_vector_type(16)));
typedef __bf16 v8bf __attribute__((ext_vector_type(8)));
typedef __bf16 v4bf __attribute__((ext_vector_type(4)));
typedef __bf16 v2bf __attribute__((ext_vector_type(2)));
#define LAS __attribute__((address_space(3)))
#define GAS __attribute__((address_space(1)))
typedef LAS char* lptr;

#define MAP 1
#define SLP_O 10
#define SLP_X 3
#define KVPOS 4
#define HKPOS 28
#define X0A 3
#define X0B 5
constexpr int CODE_KB = 44;
#define FORCE 0
#define PROF 0

constexpr u32 WS_FLAG = 4096;
constexpr u32 WS_CS = WS_FLAG + 16 * 256 * 128;
constexpr u32 WS_ST = WS_CS + 2 * 16 * 16 * 1024;
constexpr u32 WS_HK = WS_ST + 4 * 16 * 4 * 8 * 512;
constexpr u32 WS_KV = WS_HK + 2 * 256 * 4096;
constexpr u32 WS_WQ = WS_KV + 2 * 256 * 3584;
constexpr u32 WS_WO = WS_WQ + 2 * 16 * 153600;
constexpr u32 WS_X = WS_WO + 2 * 8 * 102400;
constexpr u32 WS_O = WS_X + 5 * 64 * 40960;
constexpr u32 WS_DBG = WS_O + 5 * 128 * 20480;
constexpr u32 WS_END = WS_DBG + 256 * 4 * 64 * 8 + 4096;
constexpr int R1_STEP = 2816, R2_OFF = 112640, WIMG = 153600;
constexpr int L_CS = 157696;
constexpr int L_KVR = L_CS + 1024;
constexpr int L_WPHI = L_KVR + 3520;
constexpr int L_WO = 36864;
constexpr int L_VP = 0, L_KP = 23040;
constexpr int L_PA = 0, L_PB = 16384;
constexpr int LDS_BYTES = 163840;
constexpr int AUX_SC1 = 16;
constexpr u32 OOB = 0x80000000u;
constexpr u32 SPIN_CAP = 1u << 16;
enum { PH_F0 = 0, PH_F1 = 1, PH_FW = 2 };

struct Params {
  const u16* x0; const u16* wqA; const u16* woA; const u16* wqB; const u16* wphi; const u16* woB;
  u16* out; unsigned char* ws;
};

typedef __amdgpu_buffer_rsrc_t rsrc_t;
__device__ __forceinline__ rsrc_t mk_rsrc(const void* p, u32 bytes) { return __builtin_amdgcn_make_buffer_rsrc((void*)p, 0, bytes, 0x00020000); }
__device__ __forceinline__ v4u ld16(rsrc_t r, u32 vo, u32 so) { return __builtin_amdgcn_raw_buffer_load_b128(r, vo, so, AUX_SC1); }
__device__ __forceinline__ v2u ld8(rsrc_t r, u32 vo, u32 so) { return __builtin_amdgcn_raw_buffer_load_b64(r, vo, so, AUX_SC1); }
__device__ __forceinline__ u32 ld4(rsrc_t r, u32 vo, u32 so) { return __builtin_amdgcn_raw_buffer_load_b32(r, vo, so, AUX_SC1); }
__device__ __forceinline__ v4u ld16p(rsrc_t r, u32 vo, u32 so) { return __builtin_amdgcn_raw_buffer_load_b128(r, vo, so, 0); }
__device__ __forceinline__ v2u ld8p(rsrc_t r, u32 vo, u32 so) { return __builtin_amdgcn_raw_buffer_load_b64(r, vo, so, 0); }
__device__ __forceinline__ void st16(v4u v, rsrc_t r, u32 vo, u32 so) { __builtin_amdgcn_raw_buffer_store_b128(v, r, vo, so, AUX_SC1); }
__device__ __forceinline__ void st8(v2u v, rsrc_t r, u32 vo, u32 so) { __builtin_amdgcn_raw_buffer_store_b64(v, r, vo, so, AUX_SC1); }
__device__ __forceinline__ void st4(u32 v, rsrc_t r, u32 vo, u32 so) { __builtin_amdgcn_raw_buffer_store_b32(v, r, vo, so, AUX_SC1); }
#define DRAIN() asm volatile("s_waitcnt vmcnt(0)" ::: "memory")
#define VMWAIT(n) asm volatile("s_waitcnt vmcnt(" #n ")" ::: "memory")
#define SB() __builtin_amdgcn_sched_barrier(0)
__device__ __forceinline__ void wg_barrier() {
  asm volatile("s_waitcnt lgkmcnt(0)\n\ts_barrier" ::: "memory");
}
template <int I> struct IC { static constexpr int value = I; };
template <int I, int N, class F> __device__ __forceinline__ void sfor(F&& f) {
  if constexpr (I < N) { f(IC<I>{}); sfor<I + 1, N>(f); }
}
#define A10(p) "a" #p "0", "a" #p "1", "a" #p "2", "a" #p "3", "a" #p "4", "a" #p "5", "a" #p "6", "a" #p "7", "a" #p "8", "a" #p "9"
#define ACC_OWNED "a82", "a83", "a84", "a85", "a86", "a87", "a88", "a89", "a90", "a91", "a92", "a93", "a94", "a95", "a96", "a97", "a98", "a99", A10(10), A10(11), A10(12), A10(13), A10(14), A10(15), A10(16), A10(17), A10(18), A10(19), \
  A10(20), A10(21), A10(22), A10(23), A10(24), "a250", "a251", "a252", "a253", "a254", "a255"
constexpr int AX = 96;
template <int S> __device__ __forceinline__ void ld_frag(v4i rs, u32 voff, u32 sbase) {
  u32 t;
  asm volatile("s_add_u32 %0, %3, %c6\n\tbuffer_load_dwordx4 a[%c4:%c5], %1, %2, %0 offen sc1"
               : "=&s"(t) : "v"(voff), "s"(rs), "s"(sbase), "i"(AX + 4 * S), "i"(AX + 4 * S + 3), "i"(S * 1024) : "scc", ACC_OWNED);
}
template <int N> __device__ __forceinline__ void vmwait() { asm volatile("s_waitcnt vmcnt(%c0)" :: "i"(N) : "memory"); }
template <int N> __device__ __forceinline__ void wait4(v4u (&v)[4]) {
  asm volatile("s_waitcnt vmcnt(%c4)" : "+v"(v[0]), "+v"(v[1]), "+v"(v[2]), "+v"(v[3]) : "i"(N));
}
template <int S, bool FIRST> __device__ __forceinline__ void mfma32_x(v16f& c, v8bf a) {
  if constexpr (FIRST) asm volatile("v_mfma_f32_32x32x16_bf16 %0, %1, a[%c2:%c3], 0" : "=&v"(c) : "v"(a), "i"(AX + 4 * S), "i"(AX + 4 * S + 3));
  else asm volatile("v_mfma_f32_32x32x16_bf16 %0, %1, a[%c2:%c3], %0" : "+v"(c) : "v"(a), "i"(AX + 4 * S), "i"(AX + 4 * S + 3));
}
template <int S, bool FIRST> __device__ __forceinline__ void mfma16_x(v4f& c, v8bf a) {
  if constexpr (FIRST) asm volatile("v_mfma_f32_16x16x32_bf16 %0, %1, a[%c2:%c3], 0" : "=&v"(c) : "v"(a), "i"(AX + 4 * S), "i"(AX + 4 * S + 3));
  else asm volatile("v_mfma_f32_16x16x32_bf16 %0, %1, a[%c2:%c3], %0" : "+v"(c) : "v"(a), "i"(AX + 4 * S), "i"(AX + 4 * S + 3));
}
template <bool FIRST, class TA, class TB> __device__ __forceinline__ void mfma32_v(v16f& c, TA a, TB b) {
  if constexpr (FIRST) asm volatile("s_nop 1\n\tv_mfma_f32_32x32x16_bf16 %0, %1, %2, 0" : "=&v"(c) : "v"(a), "v"(b));
  else asm volatile("s_nop 1\n\tv_mfma_f32_32x32x16_bf16 %0, %1, %2, %0" : "+v"(c) : "v"(a), "v"(b));
}
template <bool FIRST, class TA, class TB> __device__ __forceinline__ void mfma32_av(v16f& c, TA a, TB b) {
  if constexpr (FIRST) asm volatile("s_nop 1\n\tv_mfma_f32_32x32x16_bf16 %0, %1, %2, 0" : "=&v"(c) : "a"(a), "v"(b));
  else asm volatile("s_nop 1\n\tv_mfma_f32_32x32x16_bf16 %0, %1, %2, %0" : "+v"(c) : "a"(a), "v"(b));
}
#define MFMA_DONE1(x) asm volatile("s_nop 11" : "+v"(x))
#define MFMA_DONE2(x, y) asm volatile("s_nop 11" : "+v"(x), "+v"(y))
#define MFMA_DONE3(x, y, z) asm volatile("s_nop 11" : "+v"(x), "+v"(y), "+v"(z))
template <int Q> __device__ __forceinline__ void dma_set(u32 wo) { asm volatile("s_add_u32 m0, %0, %c1" :: "s"(wo), "i"(Q * 1024) : "scc"); }
__device__ __forceinline__ void dma_go(v4i rs, u32 vimg) { asm volatile("buffer_load_dwordx4 %0, %1, m0 offen sc1 lds" :: "v"(vimg), "s"(rs)); }
template <int Q> __device__ __forceinline__ void dma(v4i rs, u32 vimg, u32 wo) {
  asm volatile("s_add_u32 m0, %2, %c3\n\ts_nop 0\n\tbuffer_load_dwordx4 %0, %1, m0 offen sc1 lds" :: "v"(vimg), "s"(rs), "s"(wo), "i"(Q * 1024) : "scc");
}
constexpr int x0_slot(int blk) { return blk < 8 ? blk : blk - 7; }
template <int BLK, int Q> __device__ __forceinline__ void dma_x0(v4i rs, u32 voff, u32 ldsw) {
  asm volatile("s_add_u32 m0, %2, %c3\n\ts_nop 0\n\tbuffer_load_dwordx4 %0, %1, 0 offen offset:%c4 lds"
               :: "v"(voff), "s"(rs), "s"(ldsw), "i"(x0_slot(BLK) * 4096 + Q * 1024 - BLK * 128), "i"(BLK * 128) : "scc");
}
__device__ __forceinline__ void in16(v4u& d, v4i rs, u32 voff) { asm volatile("buffer_load_dwordx4 %0, %1, %2, 0 offen" : "=v"(d) : "v"(voff), "s"(rs)); }
template <int OFF> __device__ __forceinline__ void in8(v2u& d, v4i rs, u32 voff) {
  asm volatile("buffer_load_dwordx2 %0, %1, %2, 0 offen offset:%c3" : "=v"(d) : "v"(voff), "s"(rs), "i"(OFF));
}
__device__ __forceinline__ void flag_put(v4i rs, u32 voff, u32 soff = 0u) { asm volatile("buffer_load_dword a95, %0, %1, %2 offen sc1" :: "v"(voff), "s"(rs), "s"(soff) : ACC_OWNED); }
template <int N> __device__ __forceinline__ u32 flag_get() {
  u32 v;
  asm volatile("s_waitcnt vmcnt(%c1)\n\tv_accvgpr_read_b32 %0, a95" : "=v"(v) : "i"(N) : "memory");
  return v;
}
__device__ __forceinline__ v4i mk_desc(const void* p, u32 bytes) {
  const u64 a = (u64)p;
  return (v4i){(int)(u32)a, (int)(u32)(a >> 32), (int)bytes, 0x00020000};
}
template <int R, int OFF> __device__ __forceinline__ void lds_to_acc8(lptr a) {
  asm volatile("ds_read_b64 a[%c1:%c2], %0 offset:%c3" :: "v"(a), "i"(R), "i"(R + 1), "i"(OFF) : ACC_OWNED);
}
template <int I> __device__ __forceinline__ void park_load(v4i rs, u32 voff, u32 img) {
  u32 t;
  asm volatile("s_add_u32 %0, %3, %c6\n\tbuffer_load_dwordx4 a[%c4:%c5], %1, %2, %0 offen sc1"
               : "=&s"(t) : "v"(voff), "s"(rs), "s"(img), "i"(176 + 4 * I), "i"(179 + 4 * I), "i"((36 + 4 * I) * 1024) : "scc", ACC_OWNED);
}
template <int I> __device__ __forceinline__ void park_store(lptr a) {
  asm volatile("ds_write_b128 %0, a[%c1:%c2] offset:%c3" :: "v"(a), "i"(176 + 4 * I), "i"(179 + 4 * I), "i"((I % 10) * 4096) : "memory");
}
template <int I> __device__ __forceinline__ void halo_load(v4i rs, u32 voff, u32 base) {
  asm volatile("buffer_load_dwordx4 a[%c3:%c4], %0, %1, %2 offen offset:%c5 sc1"
               :: "v"(voff), "s"(rs), "s"(base), "i"(176 + 4 * I), "i"(179 + 4 * I), "i"(I * 1024) : ACC_OWNED);
}
template <int R> __device__ __forceinline__ void acc_write(u32 v) { asm volatile("v_accvgpr_write_b32 a%c1, %0" :: "v"(v), "i"(R) : ACC_OWNED); }
template <int R> __device__ __forceinline__ void acc_store16(v4i rs, u32 voff, u32 soff) {
  asm volatile("buffer_store_dwordx4 a[%c3:%c4], %0, %1, %2 offen sc1" :: "v"(voff), "s"(rs), "s"(soff), "i"(R), "i"(R + 3) : "memory");
}
template <int R> __device__ __forceinline__ u32 acc_read() { u32 v; asm volatile("v_accvgpr_read_b32 %0, a%c1" : "=v"(v) : "i"(R)); return v; }
template <int OFF> __device__ __forceinline__ void ld16_asm(v4u& d, v4i rs, u32 voff, u32 soff) {
  asm volatile("buffer_load_dwordx4 %0, %1, %2, %3 offen offset:%c4 sc1" : "=v"(d) : "v"(voff), "s"(rs), "s"(soff), "i"(OFF));
}
template <int STEP> __device__ __forceinline__ void ld16_step(v4u& d, v4i rs, u32 voff, u32 soff) {
  u32 t;
  asm volatile("s_add_u32 %1, %4, %c5\n\tbuffer_load_dwordx4 %0, %2, %3, %1 offen sc1" : "=v"(d), "=&s"(t) : "v"(voff), "s"(rs), "s"(soff), "i"(STEP) : "scc");
}
constexpr int WIN = 12;
constexpr int p1_nx(int s) { return s < 4 ? 2 : s < 24 ? 1 : 0; }
constexpr int p1_issued(int s) { int n = WIN; for (int t = 0; t < s; ++t) n += p1_nx(t); return n; }
constexpr int p1_pre(int s) { return (s >= 26 && s < 32) ? 1 : 0; }
constexpr int p1_post(int s) { return s == 0 ? 1 : (s >= 5 && s <= 23 && (s & 1)) ? 1 : (s >= 18 && s <= 24 && !(s & 1)) ? 1 : 0; }
constexpr int p1_wait(int s) {
  int t0 = -1;
  while (p1_issued(t0 + 1) <= s) ++t0;
  int n = p1_issued(t0 + 1) - 1 - s;
  if (t0 >= 0 && t0 < s) n += p1_post(t0);
  for (int t = t0 + 1; t <= s; ++t) n += p1_pre(t) + p1_nx(t) + (t < s ? p1_post(t) : 0);
  return n;
}
constexpr int pp_ops(int s) { return s < 4 ? 2 : (s & 3) == 0 ? 2 : 1; }
constexpr int pp_wait(int s) {
  int n = s < WIN ? WIN - 1 - s : 1 - ((s - WIN) & 1);
  for (int t = (s < WIN ? 0 : (s - WIN) / 2 + 1); t < s; ++t) n += pp_ops(t);
  return n;
}
constexpr int pp_slot(int s, int k) { int n = 0; for (int t = 4; t < s; ++t) n += pp_ops(t); return n + k; }
template <int C> __device__ __forceinline__ u32 addc(u32 x) { u32 r; asm volatile("v_add_u32 %0, %c2, %1" : "=v"(r) : "v"(x), "i"(C)); return r; }
template <int C> __device__ __forceinline__ u32 xorc(u32 x) { u32 r; asm volatile("v_xor_b32 %0, %c2, %1" : "=v"(r) : "v"(x), "i"(C)); return r; }
__device__ __forceinline__ u32 adds(u32 x, u32 y) { u32 r; asm volatile("v_add_u32 %0, %2, %1" : "=v"(r) : "v"(x), "s"(y)); return r; }
__device__ __forceinline__ int fresh(int x) { asm volatile("" : "+v"(x)); return x; }
#define LP(x) ((lptr)(size_t)(x))
__device__ __forceinline__ u32 pk2(float a, float b) { v2f f = {a, b}; return __builtin_bit_cast(u32, __builtin_convertvector(f, v2bf)); }
__device__ __forceinline__ float bflo(u32 d) { return __uint_as_float(d << 16); }
__device__ __forceinline__ float bfhi(u32 d) { return __uint_as_float(d & 0xffff0000u); }
__device__ __forceinline__ v8bf asbf(v4u v) { return __builtin_bit_cast(v8bf, v); }
__device__ __forceinline__ v8bf trfrag(lptr p0, lptr p1) {
  v4bf a = __builtin_amdgcn_ds_read_tr16_b64_v4bf16((LAS v4bf*)p0);
  v4bf b = __builtin_amdgcn_ds_read_tr16_b64_v4bf16((LAS v4bf*)p1);
  return __builtin_shufflevector(a, b, 0, 1, 2, 3, 4, 5, 6, 7);
}
__device__ __forceinline__ v16f mfma32(v8bf a, v8bf b, v16f c) { return __builtin_amdgcn_mfma_f32_32x32x16_bf16(a, b, c, 0, 0, 0); }
__device__ __forceinline__ v4f mfma16(v8bf a, v8bf b, v4f c) { return __builtin_amdgcn_mfma_f32_16x16x32_bf16(a, b, c, 0, 0, 0); }
__device__ __forceinline__ float dot2(u32 a, u32 b, float c) {
  return __builtin_amdgcn_fdot2_f32_bf16(__builtin_bit_cast(v2bf, a), __builtin_bit_cast(v2bf, b), c, false);
}
__device__ __forceinline__ void poll_flags(rsrc_t ws, u32 voff, bool on, u32 tag) {
  for (u32 sp = 0; sp < SPIN_CAP; ++sp) {
    u32 v = tag;
    asm volatile("" ::: "memory");
    if (on) v = ld4(ws, voff, 0);
    if (__all(v == tag)) break;
  }
  __builtin_amdgcn_fence(__ATOMIC_ACQUIRE, "wavefront");
}
__device__ __forceinline__ u32 flag_off(int ph, int cu) { return WS_FLAG + (u32)(ph * 256 + cu) * 128u; }

#define COUNT(k) do {} while (0)
#define STAMP(i) do {} while (0)

constexpr u32 SENT = 0xffffffffu;
__device__ __forceinline__ void probe(rsrc_t ws, u32 voff) {
  for (u32 sp = 0; sp < SPIN_CAP; ++sp) {
    asm volatile("" ::: "memory");
    if (__all(ld4(ws, voff, 0) != SENT)) break;
  }
  __builtin_amdgcn_fence(__ATOMIC_ACQUIRE, "wavefront");
}
__device__ __forceinline__ u32 max4(v4u v) { return max(max(v.x, v.y), max(v.z, v.w)); }
__device__ __forceinline__ void strict_wait(rsrc_t ws, u32 voff, u32 base, int n) {
#pragma unroll 1
  for (int s = 0; s < n; s += 10)
    for (u32 sp = 0; sp < SPIN_CAP; ++sp) {
      asm volatile("" ::: "memory");
      v4u v[10];
#pragma unroll
      for (int i = 0; i < 10; ++i) v[i] = ld16(ws, voff + (u32)i * 1024u, base + (u32)s * 1024u);
      SB();
      u32 m = 0;
#pragma unroll
      for (int i = 0; i < 10; ++i) m = max(m, max4(v[i]));
      if (__all(m != SENT)) break;
    }
  __builtin_amdgcn_fence(__ATOMIC_ACQUIRE, "wavefront");
}
__device__ __forceinline__ v2f pkfma(v2f a, v2f b, v2f c) { return __builtin_elementwise_fma(a, b, c); }
__device__ __forceinline__ float max3f(float a, float b, float c) { float r; asm("v_max3_f32 %0, %1, %2, %3" : "=v"(r) : "v"(a), "v"(b), "v"(c)); return r; }
__device__ __forceinline__ float min3f(float a, float b, float c) { float r; asm("v_min3_f32 %0, %1, %2, %3" : "=v"(r) : "v"(a), "v"(b), "v"(c)); return r; }
template <class H> __device__ __forceinline__ void dual_softmax(const v2f (&pv)[10], u32 (&phi)[20], H&& hook) {
  const float C = 1.4426950408889634f;
  float mx = max3f(pv[0].x, pv[0].y, pv[0].y), mn = min3f(pv[0].x, pv[0].y, pv[0].y);
  sfor<1, 10>([&](auto ic) {
    constexpr int i = ic.value;
    mx = max3f(mx, pv[i].x, pv[i].y); mn = min3f(mn, pv[i].x, pv[i].y);
    if constexpr (i % 3 == 0) { SB(); hook(IC<i / 3 - 1>{}); SB(); }
  });
  const float a = -mx * C, b = mn * C;
  v2f ep[10], en[10];
  sfor<0, 10>([&](auto ic) {
    constexpr int i = ic.value;
    ep[i] = pkfma(pv[i], (v2f){C, C}, (v2f){a, a}); en[i] = pkfma(pv[i], (v2f){-C, -C}, (v2f){b, b});
    if constexpr (i % 3 == 2) { SB(); hook(IC<3 + i / 3>{}); SB(); }
  });
  SB();
  sfor<0, 10>([&](auto ic) {
    constexpr int i = ic.value;
    ep[i].x = __builtin_amdgcn_exp2f(ep[i].x); ep[i].y = __builtin_amdgcn_exp2f(ep[i].y);
    en[i].x = __builtin_amdgcn_exp2f(en[i].x); en[i].y = __builtin_amdgcn_exp2f(en[i].y);
    SB(); hook(IC<6 + i>{}); SB();
  });
  v2f sp = ep[0], sn = en[0];
  sfor<1, 10>([&](auto ic) {
    constexpr int i = ic.value;
    sp += ep[i]; sn += en[i];
    if constexpr (i % 3 == 0) { SB(); hook(IC<15 + i / 3>{}); SB(); }
  });
  const float rp = __builtin_amdgcn_rcpf(sp.x + sp.y), rn = __builtin_amdgcn_rcpf(sn.x + sn.y);
  sfor<0, 10>([&](auto ic) {
    constexpr int i = ic.value;
    ep[i] *= (v2f){rp, rp}; en[i] *= (v2f){rn, rn};
    if constexpr (i % 2 == 1) { SB(); hook(IC<19 + i / 2>{}); SB(); }
  });
  SB();
  sfor<0, 10>([&](auto ic) {
    constexpr int i = ic.value;
    phi[i] = pk2(ep[i].x, ep[i].y); phi[10 + i] = pk2(en[i].x, en[i].y);
    if constexpr (i % 2 == 1 && i < 9) { SB(); hook(IC<24 + i / 2>{}); SB(); }
  });
}
__device__ __forceinline__ float xor32_add(float v);
__device__ __forceinline__ float xor16_add(float v);
__device__ __forceinline__ v4u row_sums(const v2u (&x)[5], u32 tag) {
  u32 one2 = 0x3f803f80u;
  asm volatile("" : "+s"(one2));
  float s1 = 0.f, s2 = 0.f;
#pragma unroll
  for (int c = 0; c < 5; ++c) { s1 = dot2(one2, x[c].x, s1); s2 = dot2(x[c].x, x[c].x, s2); s1 = dot2(one2, x[c].y, s1); s2 = dot2(x[c].y, x[c].y, s2); }
  s1 = xor32_add(xor16_add(s1)); s2 = xor32_add(xor16_add(s2));
  return (v4u){__float_as_uint(s1), tag, __float_as_uint(s2), tag};
}
__device__ __forceinline__ float xor32_add(float v) {
  auto r = __builtin_amdgcn_permlane32_swap(__float_as_uint(v), __float_as_uint(v), false, false);
  return __uint_as_float(r[0]) + __uint_as_float(r[1]);
}
__device__ __forceinline__ float xor16_add(float v) {
  auto r = __builtin_amdgcn_permlane16_swap(__float_as_uint(v), __float_as_uint(v), false, false);
  return __uint_as_float(r[0]) + __uint_as_float(r[1]);
}
__device__ __forceinline__ void feat_proj(const u32 (&t)[10], const v4u (&wf)[6], v2f (&pv)[10]) {
  const v4u b0 = {t[0], t[1], t[2], t[3]}, b1 = {t[4], t[5], t[6], t[7]}, b2 = {t[8], t[9], 0u, 0u};
  v16f X, Y;
  mfma32_av<true>(X, wf[0], b0); mfma32_av<true>(Y, wf[3], b0);
  mfma32_av<false>(X, wf[1], b1); mfma32_av<false>(Y, wf[4], b1);
  mfma32_av<false>(X, wf[2], b2); mfma32_av<false>(Y, wf[5], b2);
  MFMA_DONE2(X, Y);
#pragma unroll
  for (int i = 0; i < 8; ++i) pv[i] = (v2f){X[2 * i], X[2 * i + 1]};
  pv[8] = (v2f){Y[0], Y[1]}; pv[9] = (v2f){Y[2], Y[3]};
}

__global__ void __launch_bounds__(256, 1) fused_kernel(Params p) {
  extern __shared__ __attribute__((aligned(16))) char smem[];
  const lptr L0 = (lptr)smem;
  const int tid = threadIdx.x, lane = tid & 63, w = __builtin_amdgcn_readfirstlane(tid >> 6);
  const int b = blockIdx.x;
  const int tg = ((b & 7) >> 1) * 4 + (b >> 6), j = (b & 1) * 8 + ((b >> 3) & 7);
  const int cu = tg * 16 + j, th = j & 1, cg = j >> 1;
  const rsrc_t ws = mk_rsrc(p.ws, WS_END);
  const v4i wsd = mk_desc(p.ws, WS_END);
  const int tok = lane & 31, h = lane >> 5;
  const int t16 = lane & 15, g = lane >> 4;
  const int G = lane >> 4, q4 = (lane >> 2) & 3, p4 = lane & 3, rg = 4 * (G & 1) + p4;
  const u32 l16 = (u32)lane * 16u, w1k = (u32)w * 1024u, lw16 = l16 + w1k;
#define DMA_RUN(vimg, first, n) sfor<0, n>([&](auto dc) { dma<(first) + 4 * dc.value>(wsd, vimg, w1k); })
  STAMP(0);

  constexpr int SA = 131072, SBB = SA + 11520;
  const int ns = tg < 8 ? 3 : 2, s0 = tg < 8 ? 3 * tg : 8 + 2 * tg;
  v2u xres[5];
  v4u wf[6];
  u32 E;
  const u32 o_pub = WS_O + (u32)(tg * 8 + 2 * w) * 20480u + (u32)(5 * j) * 256u;
  const u32 o_lo = (u32)(tok >> 4) * 20480u + (u32)(tok & 15) * 16u;
  const u32 x_tk = (u32)((w & 1) * 16 + t16), x_lo = ((u32)(g & 1) * 32u + x_tk) * 16u;
  const u32 x_pub = WS_X + (u32)(tg * 4 + th * 2 + (w >> 1)) * 40960u + (u32)(5 * cg) * 1024u;
  auto frag_dst = [&](int ln, int i) -> u32 {
    const u32 base = w == 3 ? (u32)(R2_OFF + (s0 + i) * 1024) : (u32)((s0 + i) * R1_STEP + w * 1024);
    return (i < ns && (w != 2 || (ln & 31) < 24)) ? base + (u32)(w == 2 ? (ln >> 5) * 24 + (ln & 31) : ln) * 16u : OOB;
  };
  auto fw_off = [&](int ln) -> u32 { return ln < 20 ? flag_off(PH_FW, (ln >> 1) * 16 + 2 * cg + (ln & 1)) : OOB; };
  const v4u sv = {SENT, SENT, SENT, SENT};
  u32 oA, oT8, oT16, xA, xT8, xT16, stS, stL, poX, poO, lT, lCS, lV, lK, hkS, hkL, lP, lA, lB, kvS, kvL, lR, lX, lY, one0;
  {
    const v4i xd = mk_desc(p.x0, 2048 * 640 * 2), dA = mk_desc(p.wqA, 640 * 1920 * 2), dB = mk_desc(p.wqB, 640 * 1920 * 2);
    const v4i doA = mk_desc(p.woA, 640 * 640 * 2), doB = mk_desc(p.woB, 640 * 640 * 2), dp = mk_desc(p.wphi, 32 * 400 * 2);
    u32 src[3], stg[3];
    v4u ca[3], cb[3], cp;
    u32 ev;
#pragma unroll
    for (int i = 0; i < 3; ++i) {
      const int n = tid + 256 * i, row = n / 15, cc = n % 15, part = cc / 5, mem = cc % 5;
      const int T = (part + 2) % 3;
      src[i] = n < 240 * ns ? (u32)((16 * s0 + row) * 3840 + part * 1280 + j * 80 + mem * 16) : OOB;
      in16(ca[i], dA, src[i]);
      stg[i] = (u32)(SA + (row >> 2) * 960 + (mem == 2 ? 512 + (row & 3) * 48 + T * 16 : (T == 2 ? 704 : T * 256) + (row & 3) * 64 + (mem - (mem > 2)) * 16));
    }
    asm volatile("buffer_load_dword %0, off, %1, 0 sc1" : "=v"(ev) : "s"(wsd));
    {
      const u64 pc = __builtin_amdgcn_s_getpc() + (u64)min((b >> 3) * 2 + (w & 1), CODE_KB - 1) * 1024u;
      asm volatile("global_load_dwordx4 a[252:255], %0, %1" :: "v"(l16), "s"(pc) : ACC_OWNED);
    }
#pragma unroll
    for (int i = 0; i < 3; ++i) in16(cb[i], dB, src[i]);
    in16(cp, dp, tid < 100 ? (u32)(j * 1600 + tid * 16) : OOB);
    const u32 xrow = (u32)(128 * tg + 32 * w) * 1280u, lw = (u32)w * 32768u;
    u32 xv[4];
#pragma unroll
    for (int q = 0; q < 4; ++q) xv[q] = xrow + (u32)(8 * q + (lane >> 3)) * 1280u + (u32)((lane & 7) ^ ((4 * q + G) & 7)) * 16u;
    auto x0_dma = [&](auto ic) { sfor<0, 4>([&](auto qc) { dma_x0<decltype(ic)::value, qc.value>(xd, xv[qc.value], lw); }); };
    SB();
    sfor<0, X0A>(x0_dma);
    {
      oA = o_lo + (u32)h * 768u; oT8 = o_lo + 512u + (u32)h * 8u; oT16 = h ? OOB : o_lo + 512u;
      xA = x_lo + (u32)h * 1024u; xT8 = x_lo + 4096u + (u32)h * 8u; xT16 = h ? OOB : x_lo + 4096u;
      stS = lane < 16 ? x_tk * 16u : OOB; stL = (u32)h * 2048u + (u32)tok * 16u;
      poX = lane < 16 ? (u32)(5 * (lane >> 1) + 4) * 1024u + (u32)((lane & 1) * 16 + 15) * 16u + 8u : OOB;
      poO = lane < 16 ? (u32)(5 * lane + 2) * 256u + 248u : OOB;
      lT = 2048u + (u32)(h * 24 + tok) * 16u; lCS = (u32)(L_CS + h * 256);
      const int rowa = h * 144 + 2 + 32 * w + tok;
      lV = (u32)(L_VP + rowa * 80); lK = (u32)(L_KP + rowa * 48);
      hkS = tok >= 16 ? (u32)(h * 16 + tok - 16) * 16u : OOB; hkL = (w == 0 && tg > 0) ? l16 : OOB;
      lP = (u32)((h * 128 + 32 * w + tok) * 64 + ((tok >> 1) & 3) * 16);
      const int hd = w >> 1, part = w & 1, mm = rg >> 1, hh = rg & 1;
      const int trow = (hd * 128 + 8 * (G >> 1) + q4) * 64, z0 = q4 >> 1;
      lA = (u32)((part ? L_PB : L_PA) + trow + (((2 * hh + (mm >> 1)) ^ (part ? 3 : 0) ^ z0) * 16) + (mm & 1) * 8);
      lB = (u32)(L_PB + trow + (((rg >> 1) ^ z0) * 16) + (rg & 1) * 8);
      kvS = (tok < 21 && (part == 0 || h == 0)) ? (u32)((hd * 5 + (part ? 4 : 2 * h)) * 21 + tok) * 16u : OOB;
      kvL = (u32)min(tid, 209) * 16u;
      const int t9 = min(tid, 209);
      lR = (u32)(L_KVR + (t9 / 21) * 352 + (t9 % 21) * 16);
      const int m = tok >> 3, hp = (tok >> 2) & 1, t = tok & 3;
      lX = (u32)(L_KVR + h * 1760 + (hp == h ? 4 * m + t : 21) * 16);
      lY = (u32)(L_KVR + h * 1760 + ((hp == h && (m == 0 || (m == 1 && t == 0))) ? 16 + 4 * m + t : 21) * 16);
      asm volatile("" : "+v"(oA), "+v"(oT8), "+v"(oT16), "+v"(xA), "+v"(xT8), "+v"(xT16), "+v"(stS), "+v"(stL), "+v"(poX), "+v"(poO), "+v"(lT), "+v"(lCS),
                   "+v"(lV), "+v"(lK), "+v"(hkS), "+v"(hkL), "+v"(lP), "+v"(lA), "+v"(lB), "+v"(kvS), "+v"(kvL), "+v"(lR), "+v"(lX), "+v"(lY));
      one0 = tid == 0 ? 0u : OOB;
      asm volatile("" : "+v"(one0));
    }
    STAMP(60);
    asm volatile("s_waitcnt vmcnt(%c8)" : "+v"(ca[0]), "+v"(ca[1]), "+v"(ca[2]), "+v"(cb[0]), "+v"(cb[1]), "+v"(cb[2]), "+v"(cp), "+v"(ev) : "i"(4 * X0A));
    *(LAS v4u*)(L0 + stg[0]) = ca[0]; *(LAS v4u*)(L0 + stg[1]) = ca[1];
    *(LAS v4u*)(L0 + 11520 + stg[0]) = cb[0]; *(LAS v4u*)(L0 + 11520 + stg[1]) = cb[1];
    if (tid < 208) { *(LAS v4u*)(L0 + stg[2]) = ca[2]; *(LAS v4u*)(L0 + 11520 + stg[2]) = cb[2]; }
    if (tid < 100) *(LAS v4u*)(L0 + L_WPHI + tid * 16) = cp;
    if (tid < 10) *(LAS v4u*)(L0 + L_KVR + tid * 352 + 336) = (v4u){0u, 0u, 0u, 0u};
    E = __builtin_amdgcn_readfirstlane(ev);
    wg_barrier();
    STAMP(61);
    const int wi = 2 * tg + th;
    v4u co[4];
    v2u xr[5];
    {
      const lptr tb = L0 + SA + (G >> 1) * 960 + (rg & 1) * (w == 2 ? 8 : 32) +
                      (w == 2 ? 512 + q4 * 48 + ((rg >> 1) % 3) * 16 : (w == 3 ? 704 : w * 256) + q4 * 64 + (rg >> 1) * 8);
      v4u fa[3], fB[3];
#pragma unroll
      for (int i = 0; i < 3; ++i) fa[i] = __builtin_bit_cast(v4u, trfrag(tb + i * 3840, tb + i * 3840 + 1920));
#pragma unroll
      for (int i = 0; i < 3; ++i) fB[i] = __builtin_bit_cast(v4u, trfrag(tb + 11520 + i * 3840, tb + 11520 + i * 3840 + 1920));
#pragma unroll
      for (int i = 0; i < 3; ++i) st16(fa[i], ws, frag_dst(lane, i), WS_WQ + (u32)j * WIMG);
      st16(sv, ws, lw16, WS_HK + (u32)cu * 4096u);
      SB();
      u32 one2 = 0x3f803f80u;
      asm volatile("" : "+s"(one2));
      float sa = 0.f, sb = 0.f;
#pragma unroll
      for (int i = 0; i < 3; ++i)
#pragma unroll
        for (int c = 0; c < 4; ++c) { sa = dot2(one2, fa[i][c], sa); sb = dot2(one2, fB[i][c], sb); }
      sa = xor32_add(sa); sb = xor32_add(sb);
      sfor<0, 12>([&](auto ic) { acc_write<82 + ic.value>(fB[ic.value >> 2][ic.value & 3]); });
      const int T = w == 2 ? 3 : w == 3 ? 2 : w;
      const u32 idx = (u32)(((tok >> 2) & 1) * 64 + T * 16 + (tok >> 3) * 4 + (tok & 3));
      st8((v2u){__float_as_uint(h ? sb : sa), E + 1u}, ws, idx * 8u + (u32)h * (256u * 1024u), WS_CS + (u32)(j * 16 + tg) * 1024u);
      SB();
      sfor<X0A, X0B>(x0_dma);
      STAMP(62);
      vmwait<1 + 4 * (X0B - X0A)>();
      wg_barrier();
      st4(E + 1u, ws, one0, flag_off(PH_F0, cu));
      STAMP(1);
      sfor<X0B, 8>(x0_dma);
#pragma unroll
      for (int i = 0; i < 2; ++i) {
        const int n = tid + 256 * i, bc = n >> 4, blk = bc / 5, c = bc % 5, i8 = (n >> 1) & 7;
        const int k = 32 * wi + 8 * (2 * (blk & 1) + (i8 >> 2)) + 4 * (blk >> 1) + (i8 & 3);
        const u32 o = (wi < 20 && n < 320) ? (u32)(k * 1280 + (80 * cg + 16 * c) * 2 + (n & 1) * 16) : OOB;
        in16(co[2 * i], doA, o); in16(co[2 * i + 1], doB, o);
      }
      const u32 xo = (u32)((128 * tg + 64 * th + 16 * w + t16) * 1280 + (80 * cg + 4 * g) * 2);
      sfor<0, 5>([&](auto ic) { in8<32 * ic.value>(xr[ic.value], xd, xo); });
    }
    const lptr xa = L0 + lw + tok * 128 + h * 8;
    lptr xc[8];
#pragma unroll
    for (int c = 0; c < 8; ++c) xc[c] = xa + (c ^ ((tok >> 1) & 7)) * 16;
    auto x0_frags = [&](auto ic) {
      sfor<0, 8>([&](auto cc_) { lds_to_acc8<AX + 16 * decltype(ic)::value + 2 * cc_.value, x0_slot(decltype(ic)::value) * 4096>(xc[cc_.value]); });
    };
    static_assert(X0A >= 3 || X0B >= 3, "blocks 1 and 2 are read first");
    STAMP(58);
    vmwait<10 + 4 * (8 - 3)>();
    sfor<0, 3>(x0_frags);
    asm volatile("s_waitcnt lgkmcnt(0)" ::: "memory");
    sfor<8, 10>(x0_dma);
    STAMP(19);
    {
      const int r = lane & 31, m = r >> 3, hp = (r >> 2) & 1, t = r & 3;
#pragma unroll
      for (int y = 0; y < 2; ++y)
#pragma unroll
        for (int i = 0; i < 3; ++i) {
          const int f = y ? 16 + t : 4 * m + t;
          const bool ok = (hp == h) && (y == 0 || m == 0);
          u32 dw[4];
#pragma unroll
          for (int q = 0; q < 4; ++q) {
            u32 lo = 0, hi = 0;
            const int e0 = 8 * i + 2 * q;
            if (e0 < 20) {
              const int d0 = h ? (e0 + 4) % 20 : e0, d1 = h ? (e0 + 5) % 20 : e0 + 1;
              lo = *(LAS u16*)(L0 + L_WPHI + (h * 400 + d0 * 20 + f) * 2);
              hi = *(LAS u16*)(L0 + L_WPHI + (h * 400 + d1 * 20 + f) * 2);
            }
            dw[q] = ok ? (lo | (hi << 16)) : 0u;
          }
          wf[y * 3 + i] = (v4u){dw[0], dw[1], dw[2], dw[3]};
        }
      asm volatile("" : "+a"(wf[0]), "+a"(wf[1]), "+a"(wf[2]), "+a"(wf[3]), "+a"(wf[4]), "+a"(wf[5]));
    }
    SB();
    asm volatile("s_waitcnt vmcnt(8)" : "+v"(co[0]), "+v"(co[1]), "+v"(co[2]), "+v"(co[3]), "+v"(xr[0]), "+v"(xr[1]), "+v"(xr[2]), "+v"(xr[3]), "+v"(xr[4]));
    STAMP(24);
    const u32 f0o = lane < 16 ? flag_off(PH_F0, lane * 16 + j) : OOB;
    flag_put(wsd, f0o);
    *(LAS v4u*)(L0 + SA + tid * 16) = co[0]; *(LAS v4u*)(L0 + SA + 5120 + tid * 16) = co[1];
    if (tid < 64) { *(LAS v4u*)(L0 + SA + 4096 + tid * 16) = co[2]; *(LAS v4u*)(L0 + SA + 9216 + tid * 16) = co[3]; }
    sfor<3, 8>(x0_frags);
    {
#pragma unroll
      for (int c = 0; c < 5; ++c) xres[c] = xr[c];
      st16(row_sums(xres, E + 1u), ws, stS,
           WS_ST + (u32)((tg * 4 + th * 2 + (w >> 1)) * 8 + cg) * 512u);
    }
    SB();
    const bool f0ok = __all(lane >= 16 || flag_get<1>() == E + 1u);
    sfor<8, 10>(x0_frags);
    if (__builtin_expect(!f0ok, 0)) { COUNT(28); poll_flags(ws, f0o, lane < 16, E + 1u); }
    wg_barrier();
    STAMP(2);
    DMA_RUN(WS_WQ + (u32)j * WIMG + l16, 0, 9);
    const u32 cso = WS_CS + (u32)(((tid >> 7) * 16 + j) * 16) * 1024u + (u32)(tid & 127) * 8u;
    v2u csg[16];
#pragma unroll
    for (int i = 0; i < 16; ++i) csg[i] = ld8(ws, cso + i * 1024u, 0);
    SB();
    {
      const lptr ob = L0 + SA + (G >> 1) * 1280 + ((G & 1) * 4 + q4) * 32 + p4 * 8;
#pragma unroll
      for (int i = 0; i < 3; ++i) {
        const int f = min(w + 4 * i, 9), t2 = f >= 5, c = f - 5 * t2;
        const lptr a = ob + t2 * 5120 + c * 256;
        st16(__builtin_bit_cast(v4u, trfrag(a, a + 2560)), ws, wi < 20 ? l16 : OOB, WS_WO + (u32)(t2 * 8 + cg) * 102400u + (u32)wi * 5120u + (u32)c * 1024u);
      }
    }
    SB();
    bool ok = true;
#pragma unroll
    for (int i = 0; i < 16; ++i) ok &= csg[i].y == E + 1u;
    if (__builtin_expect(!__all(ok), 0)) {
      COUNT(29);
      for (u32 sp = 0; sp < SPIN_CAP; ++sp) {
        asm volatile("" ::: "memory");
#pragma unroll
        for (int i = 0; i < 16; ++i) csg[i] = ld8(ws, cso + i * 1024u, 0);
        SB();
        ok = true;
#pragma unroll
        for (int i = 0; i < 16; ++i) ok &= csg[i].y == E + 1u;
        if (__all(ok)) break;
      }
    }
    float s = 0.f;
#pragma unroll
    for (int i = 0; i < 16; ++i) s += __uint_as_float(csg[i].x);
    *(LAS float*)(L0 + L_CS + tid * 4) = s;
    wg_barrier();
  }
  STAMP(3);

  v4u st_late = {0u, 0u, 0u, 0u};
#pragma unroll 1
  for (int L = 0;; ++L) {
    const int ty = L & 1;
    const u32 tagL = E + 1u + (u32)L;
    const u32 imgN = WS_WQ + (u32)((ty ^ 1) * 16 + j) * WIMG;
    const u32 xb = WS_X + (u32)((L * 16 + tg) * 4 + w) * 40960u;
    const u32 sto = WS_ST + (u32)(((L * 16 + tg) * 4 + w) * 8) * 512u;
    const u32 on = o_pub + (u32)(L + 1) * (128u * 20480u), xn = x_pub + (u32)(L + 1) * (64u * 40960u);
    u32 vW;
    const LAS v4f* cs;
    v16f aK, aV, aT, aQ;
    v4u st[4];
    v4f cc[11];
    v2f nmean, rstd;
    u32 stbad;
    auto ln_stats = [&](auto part) {
      if constexpr (part.value == 0) {
        stbad = ((st[0].y ^ tagL) | (st[0].w ^ tagL) | (st[1].y ^ tagL)) | ((st[1].w ^ tagL) | (st[2].y ^ tagL) | (st[2].w ^ tagL)) | ((st[3].y ^ tagL) | (st[3].w ^ tagL));
      } else if constexpr (part.value == 1) {
        nmean.x = (__uint_as_float(st[0].x) + __uint_as_float(st[1].x)) + (__uint_as_float(st[2].x) + __uint_as_float(st[3].x));
        nmean.y = (__uint_as_float(st[0].z) + __uint_as_float(st[1].z)) + (__uint_as_float(st[2].z) + __uint_as_float(st[3].z));
        nmean.x = xor32_add(nmean.x); nmean.y = xor32_add(nmean.y);
      } else {
        const float mean = nmean.x * (1.f / 640.f);
        const float r = __builtin_amdgcn_rsqf(fmaxf(nmean.y * (1.f / 640.f) - mean * mean, 0.f) + 1e-5f);
        nmean = (v2f){-mean, -mean}; rstd = (v2f){r, r};
      }
    };
    lptr m0, m1, t0, t1;
    v4u f0[2], f1[2], f2[2];
    auto p1_begin = [&]() {
      vW = adds(l16, WS_WQ + (u32)((L & 1) * 16 + j) * WIMG);
      cs = (const LAS v4f*)LP(adds(lCS, (u32)(L & 1) * 512u));
      m0 = LP(l16); m1 = LP(addc<20 * R1_STEP>(l16)); t0 = LP(lT); t1 = LP(addc<20 * R1_STEP>(lT));
#pragma unroll
      for (int i = 0; i < 2; ++i) { f0[i] = *(LAS v4u*)(m0 + i * R1_STEP); f1[i] = *(LAS v4u*)(m0 + i * R1_STEP + 1024); f2[i] = *(LAS v4u*)(t0 + i * R1_STEP); }
      SB();
    };
    auto p1_step = [&](auto fc, auto ic) {
      constexpr int s = decltype(ic)::value, n = s + 2;
      constexpr bool FIRST = decltype(fc)::value;
      const lptr mb = (n < 20 ? m0 : m1) + (n % 20) * R1_STEP, tb = (n < 20 ? t0 : t1) + (n % 20) * R1_STEP;
      if constexpr (s == 26) st16(sv, ws, oA, on);
      if constexpr (s == 27) st16(sv, ws, oA, on + 256u);
      if constexpr (s == 28) st16(sv, ws, oT16, on);
      if constexpr (s == 29) st16(sv, ws, xA, xn);
      if constexpr (s == 30) st16(sv, ws, xA, xn + 2048u);
      if constexpr (s == 31) st16(sv, ws, xT16, xn);
      SB();
      constexpr int QA = FIRST ? (s < 10 ? 36 + 8 * s : s < 19 ? 112 + 4 * (s - 10) : s == 19 ? 146 : -1) : -1;
      constexpr int QB = FIRST ? (s < 9 ? 40 + 8 * s : -1) : ((s >= 5 && s <= 23 && (s & 1)) ? (s < 23 ? 112 + 2 * (s - 5) : 146) : -1);
      if constexpr (!FIRST) {
        sfor<p1_issued(s), p1_issued(s + 1)>([&](auto fi) { ld_frag<fi.value>(wsd, l16, xb); });
        vmwait<p1_wait(s)>();
      } else {
        if constexpr (s == 11 || s == 18 || s == 26) {
          if constexpr (s == 11) STAMP(4);
          vmwait<(s == 11 ? 15 : s == 18 ? 19 : 14)>();
          asm volatile("s_barrier" ::: "memory");
          if constexpr (s == 11) st4(E + 1u, ws, one0, flag_off(PH_FW, cu));
          STAMP(s == 11 ? 5 : s == 18 ? 11 : 63);
        }
        if constexpr (s >= 32 && s < 35) acc_store16<82 + 4 * (s - 32)>(wsd, frag_dst(fresh(lane), s - 32), imgN);
        if constexpr (s == 35) st16(sv, ws, oA, o_pub);
        if constexpr (s == 36) st16(sv, ws, oA, o_pub + 256u);
        if constexpr (s == 37) st16(sv, ws, oT16, o_pub);
        if constexpr (s == 38) st16(sv, ws, lw16, WS_HK + (u32)(256 + cu) * 4096u);
        if constexpr (s == 24 || s == 25) st16(sv, ws, fresh(tid) < 224 ? lw16 : OOB, WS_KV + (u32)(((s - 24) * 16 + j) * 16 + tg) * 3584u);
        if constexpr (s == 23) flag_put(wsd, fw_off(fresh(lane)));
      }
      if constexpr (QA >= 0) dma_set<QA>(w1k);
      SB();
      mfma32_x<s, s == 0>(aK, asbf(f0[s & 1]));
      if constexpr (QA >= 0) dma_go(wsd, vW);
      SB();
      if constexpr (n < 40) f0[s & 1] = *(LAS v4u*)mb;
      if constexpr (s == 33) { wait4<(FIRST ? 16 : 6)>(st); ln_stats(IC<0>{}); }
      if constexpr (s == 34) ln_stats(IC<1>{});
      if constexpr (s == 35) ln_stats(IC<2>{});
      if constexpr (QB >= 0) dma_set<QB>(w1k);
      SB();
      mfma32_x<s, s == 0>(aV, asbf(f1[s & 1]));
      if constexpr (QB >= 0) dma_go(wsd, vW);
      if constexpr (s == 0 && !FIRST)
        st16(st_late, ws, stS, WS_ST + (u32)(((L * 16 + tg) * 4 + th * 2 + (w >> 1)) * 8 + cg) * 512u);
      if constexpr (!FIRST && s >= 18 && s <= 24 && !(s & 1)) ld16_asm<(s - 18) * 256>(st[(s - 18) / 2], wsd, stL, sto);
      if constexpr (FIRST && s >= 11 && s < 15) ld16_asm<(s - 11) * 512>(st[s - 11], wsd, stL, sto);
      SB();
      if constexpr (n < 40) f1[s & 1] = *(LAS v4u*)(mb + 1024);
      SB();
      mfma32_x<s, s == 0>(aT, asbf(f2[s & 1]));
      SB();
      if constexpr (n < 40) f2[s & 1] = *(LAS v4u*)tb;
      if constexpr (s >= 24 && s < 35) cc[s - 24] = cs[s - 24 < 8 ? s - 24 : s - 20];
      SB();
    };
    auto p1_end = [&]() {
      asm volatile("s_nop 10" : "+v"(aK), "+v"(aV), "+v"(aT), "+v"(cc[0]), "+v"(cc[1]), "+v"(cc[2]), "+v"(cc[3]), "+v"(cc[4]), "+v"(cc[5]),
                   "+v"(cc[6]), "+v"(cc[7]), "+v"(cc[8]), "+v"(cc[9]), "+v"(cc[10]));
    };
    if (L == 0) {
      p1_begin();
      sfor<0, 40>([&](auto ic) { p1_step(IC<1>{}, ic); });
      p1_end();
      if (__builtin_expect(!__all(fresh(lane) >= 20 || flag_get<15>() == E + 1u), 0)) {
        COUNT(30);
        poll_flags(ws, fw_off(fresh(lane)), fresh(lane) < 20, E + 1u);
      }
    } else {
      for (u32 sp = 0; __builtin_expect(!__all(flag_get<WIN>() != SENT), 0) && sp < SPIN_CAP; ++sp) {
        COUNT(8 + L);
        flag_put(wsd, poX, xb);
        sfor<0, WIN>([&](auto ic) { ld_frag<ic.value>(wsd, l16, xb); });
      }
      STAMP(4 + L * 14 + 0);
#pragma unroll 1
      for (int att = 0; att < 2; ++att) {
        p1_begin();
        sfor<0, WIN>([&](auto ic) { p1_step(IC<0>{}, ic); });
        asm volatile("s_nop 9" : "+v"(aK));
        if (__builtin_expect(att == 1 || (!__any(aK[0] != aK[0]) && !(FORCE == 1 && w == (j & 3))), 1)) {
          sfor<WIN, 40>([&](auto ic) { p1_step(IC<0>{}, ic); });
          p1_end();
          if (__builtin_expect(att == 1 || (!__any(aK[0] != aK[0]) && !(FORCE == 2 && w == (j & 3))), 1)) break;
        }
        COUNT(0 + L);
        strict_wait(ws, l16, xb, 40);
        sfor<0, WIN>([&](auto ic) { ld_frag<ic.value>(wsd, l16, xb); });
      }
    }
    STAMP(4 + L * 14 + 2);
    if (__builtin_expect(__any(stbad != 0u) || (FORCE == 4 && w == (j & 3)), 0)) {
      COUNT(24 + L);
      for (u32 sp = 0; sp < SPIN_CAP; ++sp) {
        asm volatile("" ::: "memory");
#pragma unroll
        for (int i = 0; i < 4; ++i) st[i] = ld16(ws, stL, sto + i * 512u);
        ln_stats(IC<0>{});
        if (!__any(stbad != 0u)) break;
      }
      ln_stats(IC<1>{}); ln_stats(IC<2>{});
    }
#define LN2(acc, i, c4) (pkfma(nmean, ((i) & 1) ? (v2f){(c4).z, (c4).w} : (v2f){(c4).x, (c4).y}, (v2f){(acc)[2 * (i)], (acc)[2 * (i) + 1]}))
    v2f kk[10], vv[10], qt[2];
#pragma unroll
    for (int i = 0; i < 8; ++i) { kk[i] = LN2(aK, i, cc[i >> 1]); vv[i] = LN2(aV, i, cc[4 + (i >> 1)]); }
#pragma unroll
    for (int i = 0; i < 2; ++i) { kk[8 + i] = LN2(aT, i, cc[8]); vv[8 + i] = LN2(aT, 2 + i, cc[9]); qt[i] = LN2(aT, 4 + i, cc[10]); }
#pragma unroll
    for (int i = 0; i < 10; ++i) { kk[i] *= rstd; vv[i] *= rstd; }
    SB();
    u32 kp[10];
#pragma unroll
    for (int i = 0; i < 10; ++i) kp[i] = pk2(kk[i].x, kk[i].y);
    const u32 vO = adds(l16, WS_WO + (u32)(ty * 8 + cg) * 102400u - (u32)L_WO);
    v4f cq[4];
    auto pass2 = [&](auto&& hook) {
      const lptr bq = LP(addc<R2_OFF>(l16));
      v4u fq[4];
#pragma unroll
      for (int i = 0; i < 4; ++i) fq[i] = *(LAS v4u*)(bq + i * 1024);
      SB();
      sfor<0, 40>([&](auto ic) {
        constexpr int s = ic.value;
        if constexpr (!(s & 1)) asm volatile("" : "+v"(fq[s & 3]), "+v"(fq[(s + 1) & 3]));
        if constexpr (s % 3 == 0 && s < 36) dma_set<36 + 4 * (s / 3)>(w1k);
        mfma32_x<s, s == 0>(aQ, asbf(fq[s & 3]));
        if constexpr (s % 3 == 0 && s < 36) dma_go(wsd, vO);
        SB();
        if constexpr (s + 4 < 40) fq[s & 3] = *(LAS v4u*)(bq + (s + 4) * 1024);
        if constexpr (s >= 34 && s < 38) cq[s - 34] = cs[8 + s - 34];
        hook(ic);
        SB();
      });
    };
    auto make_q = [&](float qs, u32 (&qp)[10]) {
      asm volatile("s_nop 11" : "+v"(aQ), "+v"(cq[0]), "+v"(cq[1]), "+v"(cq[2]), "+v"(cq[3]));
      v2f qq[10];
#pragma unroll
      for (int i = 0; i < 8; ++i) qq[i] = LN2(aQ, i, cq[i >> 1]);
#pragma unroll
      for (int i = 0; i < 8; ++i) qq[i] *= (v2f){qs, qs};
      qq[8] = qt[0] * (v2f){qs, qs}; qq[9] = qt[1] * (v2f){qs, qs};
      SB();
#pragma unroll
      for (int i = 0; i < 10; ++i) qp[i] = pk2(qq[i].x, qq[i].y);
    };
    v2f oo[10];
    if (ty == 0) {
      if (w == 3 && tg < 15) {
        const u32 hb = WS_HK + (u32)((L >> 1) * 256 + cu) * 4096u;
#pragma unroll
        for (int i = 0; i < 5; ++i)
          st16(__builtin_bit_cast(v4u, __builtin_shufflevector(vv[2 * i], vv[2 * i + 1], 0, 1, 2, 3)), ws, hkS, hb + i * 512u);
        st16((v4u){kp[0], kp[1], kp[2], kp[3]}, ws, hkS, hb + 2560u);
        st16((v4u){kp[4], kp[5], kp[6], kp[7]}, ws, hkS, hb + 3072u);
        st16((v4u){kp[8], kp[9], 0u, 0u}, ws, hkS, hb + 3584u);
      }
      asm volatile("s_barrier" ::: "memory");
      STAMP(4 + L * 14 + 3);
      const u32 hkb = WS_HK + (u32)((L >> 1) * 256 + cu - 16) * 4096u;
      {
        LAS v4f* vp = (LAS v4f*)(LP(lV) + 14 * 80);
        LAS v4u* kq = (LAS v4u*)(LP(lK) + 14 * 48);
        pass2([&](auto ic) {
          constexpr int s = ic.value;
          if constexpr (s % 3 == 1 && s < 15) vp[s / 3] = __builtin_shufflevector(vv[2 * (s / 3)], vv[2 * (s / 3) + 1], 0, 1, 2, 3);
          if constexpr (s == 16) kq[0] = (v4u){kp[0], kp[1], kp[2], kp[3]};
          if constexpr (s == 19) kq[1] = (v4u){kp[4], kp[5], kp[6], kp[7]};
          if constexpr (s == 22) *(LAS v2u*)(kq + 2) = (v2u){kp[8], kp[9]};
          if constexpr (s >= HKPOS && s < HKPOS + 4) halo_load<s - HKPOS>(wsd, hkL, hkb);
        });
      }
      STAMP(4 + L * 14 + 4);
      u32 qp[10];
      make_q(rstd.x * (0.22360679774997896f * 1.4426950408889634f), qp);
      vmwait<16>();
      wg_barrier();
      if (L == 0) st4(E + 1u, ws, one0, flag_off(PH_F1, cu));
      if (w == 0) {
        v4u hk[4];
        vmwait<(35 - (HKPOS + 3)) / 3>();
        sfor<0, 16>([&](auto ic) { hk[ic.value >> 2][ic.value & 3] = acc_read<176 + ic.value>(); });
        if (__builtin_expect(!__all(max(max(max4(hk[0]), max4(hk[1])), max(max4(hk[2]), max4(hk[3]))) != SENT), 0)) {
          COUNT(20 + L);
          for (u32 sp = 0; sp < SPIN_CAP; ++sp) {
            asm volatile("" ::: "memory");
#pragma unroll
            for (int i = 0; i < 4; ++i) hk[i] = ld16(ws, hkL, hkb + i * 1024u);
            if (__all(max(max(max4(hk[0]), max4(hk[1])), max(max4(hk[2]), max4(hk[3]))) != SENT)) break;
          }
        }
        const int ln = fresh(lane), row = ((ln >> 4) & 1) * 144 + (ln & 15), hh = ln >> 5;
        const lptr hv = L0 + L_VP + row * 80 + hh * 16, hq = L0 + L_KP + row * 48 + hh * 16;
        *(LAS v4u*)hv = hk[0]; *(LAS v4u*)(hv + 32) = hk[1];
        *(LAS v4u*)(hh ? hq - 16 : hv + 64) = hk[2]; *(LAS v4u*)(hq + 16) = hk[3];
      }
      STAMP(4 + L * 14 + 5);
      const lptr kr = LP(lK), vr = LP(lV);
      asm volatile("" ::: "memory");
      v4u k0[8], k1[8];
      v2u k2[8];
      v4f va[3][5];
      float sc[8];
      k0[0] = (v4u){kp[0], kp[1], kp[2], kp[3]}; k1[0] = (v4u){kp[4], kp[5], kp[6], kp[7]}; k2[0] = (v2u){kp[8], kp[9]};
      auto rdk = [&](auto ic, auto pc) {
        constexpr int i = decltype(ic)::value, q = decltype(pc)::value;
        if constexpr (i < 8) {
          if constexpr (q == 0) k0[i] = *(LAS v4u*)(kr + (7 - i) * 96);
          if constexpr (q == 1) k1[i] = *(LAS v4u*)(kr + (7 - i) * 96 + 16);
          if constexpr (q == 2) k2[i] = *(LAS v2u*)(kr + (7 - i) * 96 + 32);
        }
      };
      auto rdv = [&](auto ic, auto cc_) {
        constexpr int i = decltype(ic)::value, c = decltype(cc_)::value;
        if constexpr (i < 8) va[i % 3][c] = *(LAS v4f*)(vr + (7 - i) * 160 + c * 16);
      };
      sfor<0, 3>([&](auto pc) { rdk(IC<1>{}, pc); });
      SB();
      sfor<0, 8>([&](auto ic) {
        constexpr int i = ic.value;
        if constexpr (i > 0) asm volatile("" : "+v"(k0[i]), "+v"(k1[i]), "+v"(k2[i]));
        dma_set<84 + 4 * i>(w1k);
        float a = dot2(qp[0], k0[i].x, 0.f);
        a = dot2(qp[1], k0[i].y, a);
        SB(); rdk(IC<i + 2>{}, IC<0>{}); dma_go(wsd, vO); SB();
        a = dot2(qp[2], k0[i].z, a); a = dot2(qp[3], k0[i].w, a); a = dot2(qp[4], k1[i].x, a);
        SB(); rdk(IC<i + 2>{}, IC<1>{}); SB();
        a = dot2(qp[5], k1[i].y, a); a = dot2(qp[6], k1[i].z, a); a = dot2(qp[7], k1[i].w, a);
        SB(); rdk(IC<i + 2>{}, IC<2>{}); if constexpr (i >= 3) rdv(IC<1>{}, IC<i - 3>{}); SB();
        a = dot2(qp[8], k2[i].x, a); a = dot2(qp[9], k2[i].y, a);
        asm volatile("" : "+v"(a));
        sc[i] = a;
        if constexpr (i >= 6) { SB(); rdv(IC<2>{}, IC<2 * (i - 6)>{}); rdv(IC<2>{}, IC<2 * (i - 6) + 1>{}); }
        SB();
      });
      if (tg == 0 && w == 0) {
#pragma unroll
        for (int i = 1; i < 8; ++i) if ((fresh(lane) & 31) - 2 * i < 0) sc[i] = -1e9f * 1.4426950408889634f;
      }
      dma_set<116>(w1k);
      const float mx = fmaxf(fmaxf(fmaxf(fmaxf(sc[0], sc[1]), sc[2]), fmaxf(fmaxf(sc[3], sc[4]), sc[5])), fmaxf(sc[6], sc[7]));
      SB(); dma_go(wsd, vO); rdv(IC<2>{}, IC<4>{}); SB();
      sfor<0, 8>([&](auto ic) {
        constexpr int i = ic.value;
        sc[i] = __builtin_amdgcn_exp2f(sc[i] - mx);
        if constexpr (i < 5) { SB(); rdv(IC<3>{}, IC<i>{}); SB(); }
      });
      dma_set<120>(w1k);
      const float rs = __builtin_amdgcn_rcpf(((sc[0] + sc[1]) + (sc[2] + sc[3])) + ((sc[4] + sc[5]) + (sc[6] + sc[7])));
      SB(); dma_go(wsd, vO); SB();
      sfor<0, 8>([&](auto ic) {
        constexpr int i = ic.value;
        const v2f pp = {sc[i], sc[i]};
        if constexpr (i == 0) {
          dma_set<124>(w1k);
#pragma unroll
          for (int e = 0; e < 10; ++e) oo[e] = pp * vv[e];
          SB(); dma_go(wsd, vO); SB();
        } else {
          asm volatile("" : "+v"(va[i % 3][0]), "+v"(va[i % 3][1]), "+v"(va[i % 3][2]), "+v"(va[i % 3][3]), "+v"(va[i % 3][4]));
          if constexpr (i < 3) dma_set<124 + 4 * i>(w1k);
          sfor<0, 5>([&](auto cc_) {
            constexpr int c = cc_.value;
            oo[2 * c] = pkfma(pp, va[i % 3][c].xy, oo[2 * c]); oo[2 * c + 1] = pkfma(pp, va[i % 3][c].zw, oo[2 * c + 1]);
            SB(); rdv(IC<i + 3>{}, cc_); if constexpr (i < 3 && c == 0) dma_go(wsd, vO); SB();
          });
        }
      });
#pragma unroll
      for (int e = 0; e < 10; ++e) oo[e] *= (v2f){rs, rs};
    } else {
      {
        v2f pv[10];
        u32 phi[20], vq[10];
        feat_proj(kp, wf, pv);
#pragma unroll
        for (int i = 0; i < 10; ++i) vq[i] = pk2(vv[i].x, vv[i].y);
        dual_softmax(pv, phi, [](auto) {});
        asm volatile("s_barrier" ::: "memory");
        const lptr p0 = LP(lP), p1 = LP(xorc<16>(lP)), p2 = LP(xorc<32>(lP)), p3 = LP(xorc<48>(lP));
        *(LAS v4u*)p0 = (v4u){phi[0], phi[1], phi[2], phi[3]}; *(LAS v4u*)p1 = (v4u){phi[4], phi[5], phi[6], phi[7]};
        *(LAS v4u*)p2 = (v4u){phi[8], phi[9], phi[10], phi[11]}; *(LAS v4u*)p3 = (v4u){phi[12], phi[13], phi[14], phi[15]};
        *(LAS v4u*)(p0 + L_PB) = (v4u){vq[0], vq[1], vq[2], vq[3]}; *(LAS v4u*)(p1 + L_PB) = (v4u){vq[4], vq[5], vq[6], vq[7]};
        *(LAS v4u*)(p2 + L_PB) = (v4u){vq[8], vq[9], 0x3f80u, 0u}; *(LAS v4u*)(p3 + L_PB) = (v4u){phi[16], phi[17], phi[18], phi[19]};
        const lptr a0 = LP(lA), a1 = LP(xorc<32>(lA)) + 256, b0 = LP(lB), b1 = LP(xorc<32>(lB)) + 256;
        wg_barrier();
        v8bf fa[8], fb[8];
#pragma unroll
        for (int s = 0; s < 8; ++s) { fa[s] = trfrag(a0 + s * 1024, a1 + s * 1024); fb[s] = trfrag(b0 + s * 1024, b1 + s * 1024); }
        SB();
        v16f kv;
        sfor<0, 8>([&](auto ic) { constexpr int s = ic.value; mfma32_v<s == 0>(kv, fa[s], fb[s]); });
        MFMA_DONE1(kv);
        const u32 kb = WS_KV + (u32)(((L >> 1) * 16 + j) * 16 + tg) * 3584u;
        st16((v4u){pk2(kv[0], kv[1]), pk2(kv[2], kv[3]), pk2(kv[4], kv[5]), pk2(kv[6], kv[7])}, ws, kvS, kb);
        if ((w & 1) == 0) st16((v4u){pk2(kv[8], kv[9]), pk2(kv[10], kv[11]), pk2(kv[12], kv[13]), pk2(kv[14], kv[15])}, ws, kvS, kb + 336u);
      }
      STAMP(4 + L * 14 + 3);
      pass2([](auto) {});
      STAMP(4 + L * 14 + 4);
      const u32 kvb = WS_KV + (u32)(((L >> 1) * 16 + j) * 16) * 3584u;
      v4u pr[16];
      u32 qp[10];
      make_q(rstd.x, qp);
      DMA_RUN(vO, 84, 3);
      v2f pv[10];
      u32 phi[20];
      feat_proj(qp, wf, pv);
      DMA_RUN(vO, 96, 3);
      dual_softmax(pv, phi, [&](auto kc) {
        constexpr int k = kc.value - KVPOS;
        if constexpr (k >= 0 && k < 16)
          ld16_step<k * 3584>(pr[k], wsd, kvL, kvb);
      });
      asm volatile("" : "+v"(phi[0]), "+v"(phi[1]), "+v"(phi[2]), "+v"(phi[3]), "+v"(phi[4]), "+v"(phi[5]), "+v"(phi[6]), "+v"(phi[7]), "+v"(phi[8]), "+v"(phi[9]),
                   "+v"(phi[10]), "+v"(phi[11]), "+v"(phi[12]), "+v"(phi[13]), "+v"(phi[14]), "+v"(phi[15]), "+v"(phi[16]), "+v"(phi[17]), "+v"(phi[18]), "+v"(phi[19]));
      SB();
      STAMP(4 + L * 14 + 5);
      {
        float a[8];
        u32 sel0 = 0x00003f80u, sel1 = 0x3f800000u;
        asm volatile("" : "+s"(sel0), "+s"(sel1));
#pragma unroll
        for (int c = 0; c < 8; ++c) a[c] = 0.f;
        sfor<0, 4>([&](auto qc) {
          constexpr int i0 = 4 * qc.value;
          asm volatile("s_waitcnt vmcnt(%c4)" : "+v"(pr[i0]), "+v"(pr[i0 + 1]), "+v"(pr[i0 + 2]), "+v"(pr[i0 + 3]) : "i"(12 - i0));
#pragma unroll
          for (int i = i0; i < i0 + 4; ++i)
#pragma unroll
            for (int c = 0; c < 4; ++c) { a[2 * c] = dot2(sel0, pr[i][c], a[2 * c]); a[2 * c + 1] = dot2(sel1, pr[i][c], a[2 * c + 1]); }
          SB();
        });
        const float t = (a[0] + a[2]) + (a[4] + a[6]);
        if (__builtin_expect(__any(t != t) || (FORCE == 5 && w == (j & 3)), 0)) {
          COUNT(16 + L);
#pragma unroll
          for (int c = 0; c < 8; ++c) a[c] = 0.f;
          v4u q[16];
          for (u32 sp = 0; sp < SPIN_CAP; ++sp) {
            probe(ws, fresh(lane) < 16 ? kvb + (u32)fresh(lane) * 3584u + 188u * 16u : OOB);
#pragma unroll
            for (int i = 0; i < 16; ++i) q[i] = ld16(ws, kvL, kvb + i * 3584u);
            SB();
            u32 mxs = 0;
#pragma unroll
            for (int i = 0; i < 16; ++i) mxs = max(mxs, max4(q[i]));
            if (__all(mxs != SENT)) break;
          }
#pragma unroll
          for (int i = 0; i < 16; ++i)
#pragma unroll
            for (int c = 0; c < 4; ++c) { a[2 * c] = dot2(sel0, q[i][c], a[2 * c]); a[2 * c + 1] = dot2(sel1, q[i][c], a[2 * c + 1]); }
        }
        *(LAS v4u*)LP(lR) = (v4u){pk2(a[0], a[1]), pk2(a[2], a[3]), pk2(a[4], a[5]), pk2(a[6], a[7])};
      }
      wg_barrier();
      STAMP(4 + L * 14 + 7);
      const lptr kx = LP(lX), ky = LP(lY);
      v4u ax[5], ay[5];
#pragma unroll
      for (int i = 0; i < 5; ++i) { ay[i] = *(LAS v4u*)(ky + i * 352); ax[i] = *(LAS v4u*)(kx + i * 352); }
      SB();
      v16f X, Y;
      sfor<0, 5>([&](auto ic) {
        constexpr int i = ic.value;
        const v4u bfr = {phi[4 * i], phi[4 * i + 1], phi[4 * i + 2], phi[4 * i + 3]};
        if constexpr (i < 4) dma_set<108 + 8 * i>(w1k);
        mfma32_v<i == 0>(Y, ay[i], bfr);
        if constexpr (i < 4) dma_go(wsd, vO);
        if constexpr (i < 3) dma_set<112 + 8 * i>(w1k);
        mfma32_v<i == 0>(X, ax[i], bfr);
        if constexpr (i < 3) dma_go(wsd, vO);
      });
      MFMA_DONE2(X, Y);
      const float rd = __builtin_amdgcn_rcpf(Y[4] + 1e-6f);
#pragma unroll
      for (int i = 0; i < 8; ++i) oo[i] = (v2f){X[2 * i], X[2 * i + 1]} * (v2f){rd, rd};
      oo[8] = (v2f){Y[0], Y[1]} * (v2f){rd, rd}; oo[9] = (v2f){Y[2], Y[3]} * (v2f){rd, rd};
    }
    SB();
    {
      const u32 ob = o_pub + (u32)L * (128u * 20480u);
      st16((v4u){pk2(oo[0].x, oo[0].y), pk2(oo[1].x, oo[1].y), pk2(oo[2].x, oo[2].y), pk2(oo[3].x, oo[3].y)}, ws, oA, ob);
      st16((v4u){pk2(oo[4].x, oo[4].y), pk2(oo[5].x, oo[5].y), pk2(oo[6].x, oo[6].y), pk2(oo[7].x, oo[7].y)}, ws, oA, ob + 256u);
      st8((v2u){pk2(oo[8].x, oo[8].y), pk2(oo[9].x, oo[9].y)}, ws, oT8, ob);
    }
    SB();
    STAMP(4 + L * 14 + 8);
    if (L == 0) {
      const int ln = fresh(lane);
      poll_flags(ws, flag_off(PH_F1, ln < 16 ? tg * 16 + ln : (ln & 15) * 16 + j), ln < 32, E + 1u);
    }
    sfor<0, 8>([&](auto ic) { park_load<ic.value>(wsd, lw16, imgN); });
    vmwait<11>();
    asm volatile("s_barrier" ::: "memory");
    const lptr w0 = LP(addc<L_WO>(l16)), w1 = LP(addc<L_WO + 10 * 5120>(l16));
    const u32 vN = adds(l16, imgN);
    v2f res[10];
#pragma unroll
    for (int c = 0; c < 5; ++c) { res[2 * c] = (v2f){bflo(xres[c].x), bfhi(xres[c].x)}; res[2 * c + 1] = (v2f){bflo(xres[c].y), bfhi(xres[c].y)}; }
    asm volatile("" : "+v"(res[0]), "+v"(res[1]), "+v"(res[2]), "+v"(res[3]), "+v"(res[4]), "+v"(res[5]), "+v"(res[6]), "+v"(res[7]), "+v"(res[8]), "+v"(res[9]));
    __builtin_amdgcn_s_sleep(SLP_O);
    STAMP(4 + L * 14 + 9);
    const u32 ob = WS_O + (u32)((L * 16 + tg) * 8 + th * 4 + w) * 20480u;
    {
      flag_put(wsd, poO, ob);
      sfor<0, WIN>([&](auto ic) { ld_frag<ic.value>(wsd, l16, ob); });
      for (u32 sp = 0; __builtin_expect(!__all(flag_get<WIN>() != SENT), 0) && sp < SPIN_CAP; ++sp) {
        COUNT(12 + L);
        flag_put(wsd, poO, ob);
        sfor<0, WIN>([&](auto ic) { ld_frag<ic.value>(wsd, l16, ob); });
      }
    }
    STAMP(4 + L * 14 + 10);
    v4f ac[5];
#pragma unroll 1
    for (int att = 0; att < 2; ++att) {
      v4u fa[3][5];
#pragma unroll
      for (int c = 0; c < 5; ++c) fa[0][c] = *(LAS v4u*)(w0 + c * 1024);
#pragma unroll
      for (int c = 0; c < 5; ++c) fa[1][c] = *(LAS v4u*)(w0 + 5120 + c * 1024);
      SB();
      sfor<0, 20>([&](auto ic) {
        constexpr int s = ic.value, n = s + 2;
        const lptr wb = (n < 10 ? w0 : w1) + (n % 10) * 5120;
        asm volatile("s_waitcnt vmcnt(%c5)" : "+v"(fa[s % 3][0]), "+v"(fa[s % 3][1]), "+v"(fa[s % 3][2]), "+v"(fa[s % 3][3]), "+v"(fa[s % 3][4]) : "i"(pp_wait(s)));
        sfor<0, 5>([&](auto jc) {
          constexpr int c = jc.value;
          constexpr int q = (s >= 4 && (c == 0 || (c == 3 && pp_ops(s) == 2))) ? pp_slot(s, c == 3) : -1;
          if constexpr (q >= 11) dma_set<4 * (q - 11)>(w1k);
          mfma16_x<s, s == 0>(ac[c], asbf(fa[s % 3][c]));
          if constexpr ((c == 0 || c == 3) && s < 4) ld_frag<WIN + 2 * s + (c == 3)>(wsd, l16, ob);
          if constexpr (q >= 0 && q < 11) park_load<8 + q>(wsd, lw16, imgN);
          if constexpr (q >= 11) dma_go(wsd, vN);
          SB();
          if constexpr (n < 20) fa[n % 3][c] = *(LAS v4u*)(wb + c * 1024);
          SB();
        });
      });
      asm volatile("s_nop 7" : "+v"(ac[0]), "+v"(ac[1]), "+v"(ac[2]), "+v"(ac[3]), "+v"(ac[4]));
      if (__builtin_expect(att == 1 || (!__any(ac[0][0] != ac[0][0]) && !(FORCE == 3 && w == (j & 3))), 1)) break;
      COUNT(4 + L);
      strict_wait(ws, l16, ob, 20);
      sfor<0, WIN>([&](auto ic) { ld_frag<ic.value>(wsd, l16, ob); });
    }
    STAMP(4 + L * 14 + 11);
#pragma unroll
    for (int c = 0; c < 5; ++c) { res[2 * c] += ac[c].xy; res[2 * c + 1] += ac[c].zw; }
    SB();
    v2u xr[5];
#pragma unroll
    for (int c = 0; c < 5; ++c) { xr[c].x = pk2(res[2 * c].x, res[2 * c].y); xr[c].y = pk2(res[2 * c + 1].x, res[2 * c + 1].y); }
    if (L == 3) {
      const int ln = fresh(lane);
      GAS char* op = (GAS char*)p.out + ((128 * tg + 64 * th + 16 * w + (ln & 15)) * 1280 + (80 * cg + 4 * (ln >> 4)) * 2);
#pragma unroll
      for (int c = 0; c < 5; ++c) *(GAS v2u*)(op + 32 * c) = xr[c];
      if (b == 0) st4(E + 16u, ws, one0, 0);
      STAMP(59);
      break;
    }
    {
#pragma unroll
      for (int c = 0; c < 4; c += 2) {
        auto r0 = __builtin_amdgcn_permlane32_swap(xr[c].x, xr[c + 1].x, false, false);
        auto r1 = __builtin_amdgcn_permlane32_swap(xr[c].y, xr[c + 1].y, false, false);
        st16((v4u){r0[0], r1[0], r0[1], r1[1]}, ws, xA, xn + (u32)c * 1024u);
      }
      st8(xr[4], ws, xT8, xn);
      SB();
      asm volatile("s_barrier" ::: "memory");
      STAMP(4 + L * 14 + 12);
      const lptr pk0 = LP(addc<36864>(lw16)), pk1 = LP(addc<36864 + 40960>(lw16));
      sfor<0, 19>([&](auto ic) {
        if constexpr (ic.value == 8) vmwait<12>();
        park_store<ic.value>(ic.value < 10 ? pk0 : pk1);
      });
      __builtin_amdgcn_s_sleep(SLP_X);
      const u32 xq = xb + 64u * 40960u;
      flag_put(wsd, poX, xq);
      sfor<0, WIN>([&](auto ic) { ld_frag<ic.value>(wsd, l16, xq); });
      vmwait<4 + WIN>();
      wg_barrier();
#pragma unroll
      for (int c = 0; c < 5; ++c) xres[c] = xr[c];
      st_late = row_sums(xres, tagL + 1u);
      asm volatile("" : "+v"(st_late));
    }
    STAMP(4 + L * 14 + 13);
  }
  DRAIN();
}

extern "C" void kernel_launch(void* const* d_in, const int* in_sizes, int n_in, void* d_out, int out_size, void* d_ws,
                              size_t ws_size, hipStream_t stream) {
  Params p;
  p.x0 = (const u16*)d_in[0]; p.wqA = (const u16*)d_in[1]; p.woA = (const u16*)d_in[2];
  p.wqB = (const u16*)d_in[4]; p.wphi = (const u16*)d_in[5]; p.woB = (const u16*)d_in[6];
  p.out = (u16*)d_out; p.ws = (unsigned char*)d_ws;
  (void)hipFuncSetAttribute((const void*)fused_kernel, hipFuncAttributeMaxDynamicSharedMemorySize, LDS_BYTES);
  fused_kernel<<<256, 256, LDS_BYTES, stream>>>(p);
}
```

```cpp
#include <hip/hip_runtime.h>
#include <hip/hip_bf16.h>

typedef unsigned short u16;
typedef unsigned u32;
typedef unsigned long long u64;
typedef u32 v4u __attribute__((ext_vector_type(4)));
typedef u32 v2u __attribute__((ext_vector_type(2)));
typedef int v4i __attribute__((ext_vector_type(4)));
typedef float v4f __attribute__((ext_vector_type(4)));
typedef float v2f __attribute__((ext_vector_type(2)));
typedef float v16f __attribute__((ext_vector_type(16)));
typedef __bf16 v8bf __attribute__((ext_vector_type(8)));
typedef __bf16 v4bf __attribute__((ext_vector_type(4)));
typedef __bf16 v2bf __attribute__((ext_vector_type(2)));
#define LAS __attribute__((address_space(3)))
#define GAS __attribute__((address_space(1)))
typedef LAS char* lptr;

#define MAP 1
#define SLP_O 10
#define SLP_X 0
#define KVPOS 6
#define SEG 0
#define NX2 0
#define WINX 8
#define PREF 0
#define NUP 19
#define NQE 13
#define NPE 6
#define NPK 14
#define HKPOS 28
#define SLP_F 0
#define X0A 4
#define X0B 6
constexpr int CODE_KB = 40, CODE_BACK = 0;
#define FORCE 0
#define PROF 0

constexpr u32 WS_FLAG = 4096;
constexpr u32 WS_CS = WS_FLAG + 16 * 256 * 128;
constexpr u32 WS_ST = WS_CS + 2 * 16 * 16 * 1024;
constexpr u32 WS_HK = WS_ST + 4 * 16 * 4 * 8 * 512;
constexpr u32 WS_KV = WS_HK + 2 * 256 * 4096;
constexpr u32 WS_WQ = WS_KV + 2 * 256 * 3584;
constexpr u32 WS_WO = WS_WQ + 2 * 16 * 153600;
constexpr u32 WS_X = WS_WO + 2 * 8 * 102400;
constexpr u32 WS_O = WS_X + 5 * 64 * 40960;
constexpr u32 WS_DBG = WS_O + 5 * 128 * 20480;
constexpr u32 WS_END = WS_DBG + 256 * 4 * 64 * 8 + 4096;
constexpr int R1_STEP = 2816, R2_OFF = 112640, WIMG = 153600;
constexpr int L_CS = 157696;
constexpr int L_KVR = L_CS + 1024;
constexpr int L_WPHI = L_KVR + 3520;
constexpr int L_WO = 36864;
constexpr int L_VP = 0, L_KP = 23040;
constexpr int L_PA = 0, L_PB = 16384;
constexpr int LDS_BYTES = 163840;
constexpr int AUX_SC1 = 16;
constexpr u32 OOB = 0x80000000u;
constexpr u32 SPIN_CAP = 1u << 16;
enum { PH_F0 = 0, PH_F1 = 1, PH_F2 = 2, PH_FW = 3 };

struct Params {
  const u16* x0; const u16* wqA; const u16* woA; const u16* wqB; const u16* wphi; const u16* woB;
  u16* out; unsigned char* ws;
};

typedef __amdgpu_buffer_rsrc_t rsrc_t;
__device__ __forceinline__ rsrc_t mk_rsrc(const void* p, u32 bytes) { return __builtin_amdgcn_make_buffer_rsrc((void*)p, 0, bytes, 0x00020000); }
__device__ __forceinline__ v4u ld16(rsrc_t r, u32 vo, u32 so) { return __builtin_amdgcn_raw_buffer_load_b128(r, vo, so, AUX_SC1); }
__device__ __forceinline__ v2u ld8(rsrc_t r, u32 vo, u32 so) { return __builtin_amdgcn_raw_buffer_load_b64(r, vo, so, AUX_SC1); }
__device__ __forceinline__ u32 ld4(rsrc_t r, u32 vo, u32 so) { return __builtin_amdgcn_raw_buffer_load_b32(r, vo, so, AUX_SC1); }
__device__ __forceinline__ v4u ld16p(rsrc_t r, u32 vo, u32 so) { return __builtin_amdgcn_raw_buffer_load_b128(r, vo, so, 0); }
__device__ __forceinline__ v2u ld8p(rsrc_t r, u32 vo, u32 so) { return __builtin_amdgcn_raw_buffer_load_b64(r, vo, so, 0); }
__device__ __forceinline__ void st16(v4u v, rsrc_t r, u32 vo, u32 so) { __builtin_amdgcn_raw_buffer_store_b128(v, r, vo, so, AUX_SC1); }
__device__ __forceinline__ void st8(v2u v, rsrc_t r, u32 vo, u32 so) { __builtin_amdgcn_raw_buffer_store_b64(v, r, vo, so, AUX_SC1); }
__device__ __forceinline__ void st4(u32 v, rsrc_t r, u32 vo, u32 so) { __builtin_amdgcn_raw_buffer_store_b32(v, r, vo, so, AUX_SC1); }
#define DRAIN() asm volatile("s_waitcnt vmcnt(0)" ::: "memory")
#define VMWAIT(n) asm volatile("s_waitcnt vmcnt(" #n ")" ::: "memory")
#define SB() __builtin_amdgcn_sched_barrier(0)
__device__ __forceinline__ void wg_barrier() {
  asm volatile("s_waitcnt lgkmcnt(0)\n\ts_barrier" ::: "memory");
}
template <int I> struct IC { static constexpr int value = I; };
template <int I, int N, class F> __device__ __forceinline__ void sfor(F&& f) {
  if constexpr (I < N) { f(IC<I>{}); sfor<I + 1, N>(f); }
}
#define A10(p) "a" #p "0", "a" #p "1", "a" #p "2", "a" #p "3", "a" #p "4", "a" #p "5", "a" #p "6", "a" #p "7", "a" #p "8", "a" #p "9"
#define ACC_OWNED "a46", "a47", "a48", "a49", A10(5), A10(6), "a82", "a83", "a84", "a85", "a86", "a87", "a88", "a89", "a90", "a91", "a92", "a93", "a94", "a95", "a96", "a97", "a98", "a99", A10(10), A10(11), A10(12), A10(13), A10(14), A10(15), A10(16), A10(17), A10(18), A10(19), \
  A10(20), A10(21), A10(22), A10(23), A10(24), "a250", "a251", "a252", "a253", "a254", "a255"
constexpr int AX = 96;
template <int S> __device__ __forceinline__ void ld_frag(v4i rs, u32 voff, u32 sbase) {
  u32 t;
  asm volatile("s_add_u32 %0, %3, %c6\n\tbuffer_load_dwordx4 a[%c4:%c5], %1, %2, %0 offen sc1"
               : "=&s"(t) : "v"(voff), "s"(rs), "s"(sbase), "i"(AX + 4 * S), "i"(AX + 4 * S + 3), "i"(S * 1024) : "scc", ACC_OWNED);
}
__device__ __forceinline__ void frag_window(v4i rs, u32 voff, u32 sbase) {
  u32 t;
#define FW4(a, b, c, d) "buffer_load_dwordx4 a[" a "], %1, %2, %0 offen sc1\n\tbuffer_load_dwordx4 a[" b "], %1, %2, %0 offen offset:1024 sc1\n\t" \
                        "buffer_load_dwordx4 a[" c "], %1, %2, %0 offen offset:2048 sc1\n\tbuffer_load_dwordx4 a[" d "], %1, %2, %0 offen offset:3072 sc1\n\t"
  asm volatile("s_mov_b32 %0, %3\n\t" FW4("96:99", "100:103", "104:107", "108:111") "s_add_u32 %0, %3, 4096\n\t" FW4("112:115", "116:119", "120:123", "124:127")
               : "=&s"(t) : "v"(voff), "s"(rs), "s"(sbase) : "scc", ACC_OWNED);
}
template <int N> __device__ __forceinline__ void vmwait() { asm volatile("s_waitcnt vmcnt(%c0)" :: "i"(N) : "memory"); }
template <int N> __device__ __forceinline__ void wait3(v4u& a, v4u& b, v4u& c) {
  if constexpr (N >= 0) asm volatile("s_waitcnt vmcnt(%c3)" : "+v"(a), "+v"(b), "+v"(c) : "i"(N));
  else asm volatile("" : "+v"(a), "+v"(b), "+v"(c));
}
template <int N> __device__ __forceinline__ void wait4(v4u (&v)[4]) {
  asm volatile("s_waitcnt vmcnt(%c4)" : "+v"(v[0]), "+v"(v[1]), "+v"(v[2]), "+v"(v[3]) : "i"(N));
}
template <int S, bool FIRST> __device__ __forceinline__ void mfma32_x(v16f& c, v8bf a) {
  if constexpr (FIRST) asm volatile("v_mfma_f32_32x32x16_bf16 %0, %1, a[%c2:%c3], 0" : "=&v"(c) : "v"(a), "i"(AX + 4 * S), "i"(AX + 4 * S + 3));
  else asm volatile("v_mfma_f32_32x32x16_bf16 %0, %1, a[%c2:%c3], %0" : "+v"(c) : "v"(a), "i"(AX + 4 * S), "i"(AX + 4 * S + 3));
}
template <int S, bool FIRST, int N> __device__ __forceinline__ void mfma32_xw(v16f& c, v4u a, v4u o1, v4u o2) {
  if constexpr (FIRST && N >= 0) asm volatile("s_waitcnt vmcnt(%c4)\n\tv_mfma_f32_32x32x16_bf16 %0, %1, a[%c2:%c3], 0" : "=&v"(c) : "v"(a), "i"(AX + 4 * S), "i"(AX + 4 * S + 3), "i"(N), "v"(o1), "v"(o2));
  else if constexpr (N >= 0) asm volatile("s_waitcnt vmcnt(%c4)\n\tv_mfma_f32_32x32x16_bf16 %0, %1, a[%c2:%c3], %0" : "+v"(c) : "v"(a), "i"(AX + 4 * S), "i"(AX + 4 * S + 3), "i"(N), "v"(o1), "v"(o2));
  else if constexpr (FIRST) asm volatile("v_mfma_f32_32x32x16_bf16 %0, %1, a[%c2:%c3], 0" : "=&v"(c) : "v"(a), "i"(AX + 4 * S), "i"(AX + 4 * S + 3), "v"(o1), "v"(o2));
  else asm volatile("v_mfma_f32_32x32x16_bf16 %0, %1, a[%c2:%c3], %0" : "+v"(c) : "v"(a), "i"(AX + 4 * S), "i"(AX + 4 * S + 3), "v"(o1), "v"(o2));
}
template <int S, bool FIRST, int N> __device__ __forceinline__ void mfma16_xw(v4f& c, const v4u (&a)[5]) {
  if constexpr (FIRST) asm volatile("s_waitcnt vmcnt(%c4)\n\tv_mfma_f32_16x16x32_bf16 %0, %1, a[%c2:%c3], 0" : "=&v"(c) : "v"(a[0]), "i"(AX + 4 * S), "i"(AX + 4 * S + 3), "i"(N), "v"(a[1]), "v"(a[2]), "v"(a[3]), "v"(a[4]));
  else asm volatile("s_waitcnt vmcnt(%c4)\n\tv_mfma_f32_16x16x32_bf16 %0, %1, a[%c2:%c3], %0" : "+v"(c) : "v"(a[0]), "i"(AX + 4 * S), "i"(AX + 4 * S + 3), "i"(N), "v"(a[1]), "v"(a[2]), "v"(a[3]), "v"(a[4]));
}
template <int S, bool FIRST> __device__ __forceinline__ void mfma16_x(v4f& c, v8bf a) {
  if constexpr (FIRST) asm volatile("v_mfma_f32_16x16x32_bf16 %0, %1, a[%c2:%c3], 0" : "=&v"(c) : "v"(a), "i"(AX + 4 * S), "i"(AX + 4 * S + 3));
  else asm volatile("v_mfma_f32_16x16x32_bf16 %0, %1, a[%c2:%c3], %0" : "+v"(c) : "v"(a), "i"(AX + 4 * S), "i"(AX + 4 * S + 3));
}
template <bool FIRST, class TA, class TB> __device__ __forceinline__ void mfma32_v(v16f& c, TA a, TB b) {
  if constexpr (FIRST) asm volatile("s_nop 1\n\tv_mfma_f32_32x32x16_bf16 %0, %1, %2, 0" : "=&v"(c) : "v"(a), "v"(b));
  else asm volatile("s_nop 1\n\tv_mfma_f32_32x32x16_bf16 %0, %1, %2, %0" : "+v"(c) : "v"(a), "v"(b));
}
template <bool FIRST, int R> __device__ __forceinline__ void mfma32_av(v16f& c, v4u b) {
  if constexpr (FIRST) asm volatile("s_nop 1\n\tv_mfma_f32_32x32x16_bf16 %0, a[%c2:%c3], %1, 0" : "=&v"(c) : "v"(b), "i"(R), "i"(R + 3));
  else asm volatile("s_nop 1\n\tv_mfma_f32_32x32x16_bf16 %0, a[%c2:%c3], %1, %0" : "+v"(c) : "v"(b), "i"(R), "i"(R + 3));
}
template <bool FIRST, class TA, class TB> __device__ __forceinline__ void mfma32_p(v16f& c, TA a, TB b) {
  if constexpr (FIRST) asm volatile("v_mfma_f32_32x32x16_bf16 %0, %1, %2, 0" : "=&v"(c) : "v"(a), "v"(b));
  else asm volatile("v_mfma_f32_32x32x16_bf16 %0, %1, %2, %0" : "+v"(c) : "v"(a), "v"(b));
}
#define MFMA_DONE1(x) asm volatile("s_nop 11" : "+v"(x))
#define MFMA_DONE2(x, y) asm volatile("s_nop 11" : "+v"(x), "+v"(y))
#define MFMA_DONE3(x, y, z) asm volatile("s_nop 11" : "+v"(x), "+v"(y), "+v"(z))
template <int Q> __device__ __forceinline__ void dma_set(u32 wo) { asm volatile("s_add_u32 m0, %0, %c1" :: "s"(wo), "i"(Q * 1024) : "scc"); }
__device__ __forceinline__ void dma_go(v4i rs, u32 vimg) { asm volatile("buffer_load_dwordx4 %0, %1, m0 offen sc1 lds" :: "v"(vimg), "s"(rs)); }
template <int Q> __device__ __forceinline__ void dma(v4i rs, u32 vimg, u32 wo) {
  asm volatile("s_add_u32 m0, %2, %c3\n\ts_nop 0\n\tbuffer_load_dwordx4 %0, %1, m0 offen sc1 lds" :: "v"(vimg), "s"(rs), "s"(wo), "i"(Q * 1024) : "scc");
}
template <int TILE> __device__ __forceinline__ void dma_x0(v4i rs, u32 voff, u32 soff, u32 lds) {
  u32 t;
  asm volatile("s_add_u32 m0, %4, %c5\n\ts_add_u32 %0, %3, %c5\n\tbuffer_load_dwordx4 %1, %2, %0 offen lds"
               : "=&s"(t) : "v"(voff), "s"(rs), "s"(soff), "s"(lds), "i"(TILE * 40960) : "scc");
}
__device__ __forceinline__ void in16(v4u& d, v4i rs, u32 voff, u32 soff) { asm volatile("buffer_load_dwordx4 %0, %1, %2, %3 offen" : "=v"(d) : "v"(voff), "s"(rs), "s"(soff)); }
__device__ __forceinline__ void flag_put(v4i rs, u32 voff, u32 soff = 0u) {
  u32 t;
  asm volatile("s_mov_b32 %0, %3\n\tbuffer_load_dword a95, %1, %2, %0 offen sc1" : "=&s"(t) : "v"(voff), "s"(rs), "s"(soff) : ACC_OWNED);
}
template <int N> __device__ __forceinline__ u32 flag_get() {
  u32 v;
  asm volatile("s_waitcnt vmcnt(%c1)\n\tv_accvgpr_read_b32 %0, a95" : "=v"(v) : "i"(N) : "memory");
  return v;
}
__device__ __forceinline__ v4i mk_desc(const void* p, u32 bytes) {
  const u64 a = (u64)p;
  return (v4i){(int)(u32)a, (int)(u32)(a >> 32), (int)bytes, 0x00020000};
}
template <int R, int OFF> __device__ __forceinline__ void lds_to_acc(u32 a) {
  asm volatile("ds_read_b128 a[%c1:%c2], %0 offset:%c3" :: "v"(a), "i"(R), "i"(R + 3), "i"(OFF) : ACC_OWNED);
}
template <int I> __device__ __forceinline__ void park_load(v4i rs, u32 voff, u32 img) {
  u32 t;
  asm volatile("s_add_u32 %0, %3, %c6\n\tbuffer_load_dwordx4 a[%c4:%c5], %1, %2, %0 offen sc1"
               : "=&s"(t) : "v"(voff), "s"(rs), "s"(img), "i"(176 + 4 * I), "i"(179 + 4 * I), "i"((36 + 4 * I) * 1024) : "scc", ACC_OWNED);
}
template <int I> __device__ __forceinline__ void park_store(lptr a) {
  asm volatile("ds_write_b128 %0, a[%c1:%c2] offset:%c3" :: "v"(a), "i"(176 + 4 * I), "i"(179 + 4 * I), "i"((I % 10) * 4096) : "memory");
}
__device__ __forceinline__ void halo_load(v4i rs, u32 voff, u32 base) {
  u32 t;
  asm volatile("s_mov_b32 %0, %3\n\tbuffer_load_dwordx4 a[176:179], %1, %2, %0 offen sc1" : "=&s"(t) : "v"(voff), "s"(rs), "s"(base) : ACC_OWNED);
}
template <int R> __device__ __forceinline__ void tr_to_acc(lptr p0, lptr p1) {
  asm volatile("ds_read_b64_tr_b16 a[%c2:%c3], %0\n\tds_read_b64_tr_b16 a[%c4:%c5], %1" :: "v"(p0), "v"(p1), "i"(R), "i"(R + 1), "i"(R + 2), "i"(R + 3) : "memory", ACC_OWNED);
}
template <int R> __device__ __forceinline__ void acc_store16(v4i rs, u32 voff, u32 soff) {
  u32 t;
  asm volatile("s_mov_b32 %0, %3\n\tbuffer_store_dwordx4 a[%c4:%c5], %1, %2, %0 offen sc1" : "=&s"(t) : "v"(voff), "s"(rs), "s"(soff), "i"(R), "i"(R + 3) : "memory");
}
template <int R> __device__ __forceinline__ u32 acc_read() { u32 v; asm volatile("v_accvgpr_read_b32 %0, a%c1" : "=v"(v) : "i"(R)); return v; }
template <int OFF> __device__ __forceinline__ void ld16_asm(v4u& d, v4i rs, u32 voff, u32 soff) {
  u32 t;
  asm volatile("s_mov_b32 %1, %4\n\tbuffer_load_dwordx4 %0, %2, %3, %1 offen offset:%c5 sc1" : "=v"(d), "=&s"(t) : "v"(voff), "s"(rs), "s"(soff), "i"(OFF));
}
template <int STEP> __device__ __forceinline__ void ld16_step(v4u& d, v4i rs, u32 voff, u32 soff) {
  u32 t;
  asm volatile("s_add_u32 %1, %4, %c5\n\tbuffer_load_dwordx4 %0, %2, %3, %1 offen sc1" : "=v"(d), "=&s"(t) : "v"(voff), "s"(rs), "s"(soff), "i"(STEP) : "scc");
}
constexpr int WIN = WINX;
constexpr int p1_nx(int s) { return s < NX2 ? 2 : s < 40 - WIN - NX2 ? 1 : 0; }
constexpr int p1_issued(int s) { int n = WIN; for (int t = 0; t < s; ++t) n += p1_nx(t); return n; }
constexpr int p1_pre(int s) { return 0; }
constexpr int p1_post(int s) { return (s <= 27 && (s & 1)) ? 1 : 0; }
constexpr int p1_wait(int s) {
  int t0 = -1;
  while (p1_issued(t0 + 1) <= s) ++t0;
  int n = p1_issued(t0 + 1) - 1 - s;
  if (t0 >= 0) n += p1_post(t0) + p1_pre(t0);
  for (int t = t0 + 1; t < s; ++t) n += p1_pre(t) + p1_nx(t) + p1_post(t);
  return n;
}
constexpr int PP_N = 19 - NPK + 9;
constexpr int NO = 20 - WIN;
constexpr int pp_cnt(int s) { return s < NO ? 1 : (PP_N * (s - NO + 1) / WIN - PP_N * (s - NO) / WIN); }
constexpr int pp_slot(int s) { return PP_N * (s - NO) / WIN; }
constexpr int pp_wait(int s) {
  int n = s < WIN ? WIN - 1 - s : 0;
  for (int t = (s < WIN ? 0 : s - WIN + 1); t < s; ++t) n += pp_cnt(t);
  return n;
}
static_assert(PP_N <= 2 * WIN, "at most two pieces a step");
template <int C> __device__ __forceinline__ u32 addc(u32 x) { u32 r; asm volatile("v_add_u32 %0, %c2, %1" : "=v"(r) : "v"(x), "i"(C)); return r; }
template <int C> __device__ __forceinline__ u32 xorc(u32 x) { u32 r; asm volatile("v_xor_b32 %0, %c2, %1" : "=v"(r) : "v"(x), "i"(C)); return r; }
__device__ __forceinline__ u32 adds(u32 x, u32 y) { u32 r; asm volatile("v_add_u32 %0, %2, %1" : "=v"(r) : "v"(x), "s"(y)); return r; }
__device__ __forceinline__ int fresh(int x) { asm volatile("" : "+v"(x)); return x; }
#define LP(x) ((lptr)(size_t)(x))
__device__ __forceinline__ u32 pk2(float a, float b) { v2f f = {a, b}; return __builtin_bit_cast(u32, __builtin_convertvector(f, v2bf)); }
__device__ __forceinline__ float bflo(u32 d) { return __uint_as_float(d << 16); }
__device__ __forceinline__ float bfhi(u32 d) { return __uint_as_float(d & 0xffff0000u); }
__device__ __forceinline__ v8bf asbf(v4u v) { return __builtin_bit_cast(v8bf, v); }
__device__ __forceinline__ v8bf trfrag(lptr p0, lptr p1) {
  v4bf a = __builtin_amdgcn_ds_read_tr16_b64_v4bf16((LAS v4bf*)p0);
  v4bf b = __builtin_amdgcn_ds_read_tr16_b64_v4bf16((LAS v4bf*)p1);
  return __builtin_shufflevector(a, b, 0, 1, 2, 3, 4, 5, 6, 7);
}
__device__ __forceinline__ v16f mfma32(v8bf a, v8bf b, v16f c) { return __builtin_amdgcn_mfma_f32_32x32x16_bf16(a, b, c, 0, 0, 0); }
__device__ __forceinline__ v4f mfma16(v8bf a, v8bf b, v4f c) { return __builtin_amdgcn_mfma_f32_16x16x32_bf16(a, b, c, 0, 0, 0); }
__device__ __forceinline__ float dot2(u32 a, u32 b, float c) {
  return __builtin_amdgcn_fdot2_f32_bf16(__builtin_bit_cast(v2bf, a), __builtin_bit_cast(v2bf, b), c, false);
}
__device__ __forceinline__ void poll_flags(rsrc_t ws, u32 voff, bool on, u32 tag) {
  for (u32 sp = 0; sp < SPIN_CAP; ++sp) {
    u32 v = tag;
    asm volatile("" ::: "memory");
    if (on) v = ld4(ws, voff, 0);
    if (__all(v == tag)) break;
  }
  __builtin_amdgcn_fence(__ATOMIC_ACQUIRE, "wavefront");
}
__device__ __forceinline__ u32 flag_off(int ph, int cu) { return WS_FLAG + (u32)(ph * 256 + cu) * 128u; }

#define COUNT(k) do {} while (0)
#define STAMP(i) do {} while (0)

constexpr u32 SENT = 0xffffffffu;
__device__ __forceinline__ void probe(rsrc_t ws, u32 voff) {
  for (u32 sp = 0; sp < SPIN_CAP; ++sp) {
    asm volatile("" ::: "memory");
    if (__all(ld4(ws, voff, 0) != SENT)) break;
  }
  __builtin_amdgcn_fence(__ATOMIC_ACQUIRE, "wavefront");
}
__device__ __forceinline__ u32 max4(v4u v) { return max(max(v.x, v.y), max(v.z, v.w)); }
__device__ __forceinline__ void strict_wait(rsrc_t ws, u32 voff, u32 base, int n) {
#pragma unroll 1
  for (int s = 0; s < n; s += 10)
    for (u32 sp = 0; sp < SPIN_CAP; ++sp) {
      asm volatile("" ::: "memory");
      v4u v[10];
#pragma unroll
      for (int i = 0; i < 10; ++i) v[i] = ld16(ws, voff, base + (u32)(s + i) * 1024u);
      SB();
      u32 m = 0;
#pragma unroll
      for (int i = 0; i < 10; ++i) m = max(m, max4(v[i]));
      if (__all(m != SENT)) break;
    }
  __builtin_amdgcn_fence(__ATOMIC_ACQUIRE, "wavefront");
}
__device__ __forceinline__ v2f pkfma(v2f a, v2f b, v2f c) { return __builtin_elementwise_fma(a, b, c); }
__device__ __forceinline__ float max3f(float a, float b, float c) { float r; asm("v_max3_f32 %0, %1, %2, %3" : "=v"(r) : "v"(a), "v"(b), "v"(c)); return r; }
__device__ __forceinline__ float min3f(float a, float b, float c) { float r; asm("v_min3_f32 %0, %1, %2, %3" : "=v"(r) : "v"(a), "v"(b), "v"(c)); return r; }
template <class H> __device__ __forceinline__ void dual_softmax(const v2f (&pv)[10], u32 (&phi)[20], H&& hook) {
  const float C = 1.4426950408889634f;
  float mx = max3f(pv[0].x, pv[0].y, pv[0].y), mn = min3f(pv[0].x, pv[0].y, pv[0].y);
  sfor<1, 10>([&](auto ic) {
    constexpr int i = ic.value;
    mx = max3f(mx, pv[i].x, pv[i].y); mn = min3f(mn, pv[i].x, pv[i].y);
    if constexpr (i % 3 == 0) { SB(); hook(IC<i / 3 - 1>{}); SB(); }
  });
  const float a = -mx * C, b = mn * C;
  v2f ep[10], en[10];
  sfor<0, 10>([&](auto ic) {
    constexpr int i = ic.value;
    ep[i] = pkfma(pv[i], (v2f){C, C}, (v2f){a, a}); en[i] = pkfma(pv[i], (v2f){-C, -C}, (v2f){b, b});
    if constexpr (i % 3 == 2) { SB(); hook(IC<3 + i / 3>{}); SB(); }
  });
  SB();
  sfor<0, 10>([&](auto ic) {
    constexpr int i = ic.value;
    ep[i].x = __builtin_amdgcn_exp2f(ep[i].x); ep[i].y = __builtin_amdgcn_exp2f(ep[i].y);
    en[i].x = __builtin_amdgcn_exp2f(en[i].x); en[i].y = __builtin_amdgcn_exp2f(en[i].y);
    SB(); hook(IC<6 + i>{}); SB();
  });
  v2f sp = ep[0], sn = en[0];
  sfor<1, 10>([&](auto ic) {
    constexpr int i = ic.value;
    sp += ep[i]; sn += en[i];
    if constexpr (i % 3 == 0) { SB(); hook(IC<15 + i / 3>{}); SB(); }
  });
  const float rp = __builtin_amdgcn_rcpf(sp.x + sp.y), rn = __builtin_amdgcn_rcpf(sn.x + sn.y);
  sfor<0, 10>([&](auto ic) {
    constexpr int i = ic.value;
    ep[i] *= (v2f){rp, rp}; en[i] *= (v2f){rn, rn};
    if constexpr (i % 2 == 1) { SB(); hook(IC<19 + i / 2>{}); SB(); }
  });
  SB();
  sfor<0, 10>([&](auto ic) {
    constexpr int i = ic.value;
    phi[i] = pk2(ep[i].x, ep[i].y); phi[10 + i] = pk2(en[i].x, en[i].y);
    if constexpr (i % 2 == 1 && i < 9) { SB(); hook(IC<24 + i / 2>{}); SB(); }
  });
}
__device__ __forceinline__ float xor32_add(float v);
__device__ __forceinline__ float xor16_add(float v);
__device__ __forceinline__ v4u row_sums(const v2u (&x)[5], u32 tag) {
  u32 one2 = 0x3f803f80u;
  asm volatile("" : "+s"(one2));
  float s1 = 0.f, s2 = 0.f;
#pragma unroll
  for (int c = 0; c < 5; ++c) { s1 = dot2(one2, x[c].x, s1); s2 = dot2(x[c].x, x[c].x, s2); s1 = dot2(one2, x[c].y, s1); s2 = dot2(x[c].y, x[c].y, s2); }
  s1 = xor32_add(xor16_add(s1)); s2 = xor32_add(xor16_add(s2));
  return (v4u){__float_as_uint(s1), tag, __float_as_uint(s2), tag};
}
__device__ __forceinline__ float xor32_add(float v) {
  auto r = __builtin_amdgcn_permlane32_swap(__float_as_uint(v), __float_as_uint(v), false, false);
  return __uint_as_float(r[0]) + __uint_as_float(r[1]);
}
__device__ __forceinline__ float xor16_add(float v) {
  auto r = __builtin_amdgcn_permlane16_swap(__float_as_uint(v), __float_as_uint(v), false, false);
  return __uint_as_float(r[0]) + __uint_as_float(r[1]);
}
constexpr int AWF = 46;
template <class H> __device__ __forceinline__ void feat_proj(const u32 (&t)[10], v2f (&pv)[10], H&& hook) {
  const v4u b0 = {t[0], t[1], t[2], t[3]}, b1 = {t[4], t[5], t[6], t[7]}, b2 = {t[8], t[9], 0u, 0u};
  v16f X, Y;
  mfma32_av<true, AWF>(X, b0); SB(); hook(IC<0>{}); SB();
  mfma32_av<true, AWF + 12>(Y, b0); SB(); hook(IC<1>{}); SB();
  mfma32_av<false, AWF + 4>(X, b1); SB(); hook(IC<2>{}); SB();
  mfma32_av<false, AWF + 16>(Y, b1); SB(); hook(IC<3>{}); SB();
  mfma32_av<false, AWF + 20>(Y, b2); SB(); hook(IC<4>{}); SB();
  mfma32_av<false, AWF + 8>(X, b2); SB(); hook(IC<5>{}); SB();
  MFMA_DONE2(X, Y);
#pragma unroll
  for (int i = 0; i < 8; ++i) pv[i] = (v2f){X[2 * i], X[2 * i + 1]};
  pv[8] = (v2f){Y[0], Y[1]}; pv[9] = (v2f){Y[2], Y[3]};
}
template <int I> __device__ __forceinline__ void kv_load(v4i rs, u32 voff, u32 base) {
  u32 t;
  asm volatile("s_add_u32 %0, %3, %c6\n\tbuffer_load_dwordx4 a[%c4:%c5], %1, %2, %0 offen sc1"
               : "=&s"(t) : "v"(voff), "s"(rs), "s"(base), "i"(AX + 4 * I), "i"(AX + 4 * I + 3), "i"(I * 3584) : "scc", ACC_OWNED);
}

__global__ void __launch_bounds__(256, 1) fused_kernel(Params p) {
  extern __shared__ __attribute__((aligned(16))) char smem[];
  const lptr L0 = (lptr)smem;
  const int tid = threadIdx.x, lane = tid & 63, w = __builtin_amdgcn_readfirstlane(tid >> 6);
  const int b = blockIdx.x;
  const int tg = ((b & 7) >> 1) * 4 + (b >> 6), j = (b & 1) * 8 + ((b >> 3) & 7);
  const int cu = tg * 16 + j, th = j & 1, cg = j >> 1;
  const rsrc_t ws = mk_rsrc(p.ws, WS_END);
  const v4i wsd = mk_desc(p.ws, WS_END);
  const int tok = lane & 31, h = lane >> 5;
  const int t16 = lane & 15, g = lane >> 4;
  const int G = lane >> 4, q4 = (lane >> 2) & 3, p4 = lane & 3, rg = 4 * (G & 1) + p4;
  const u32 l16 = (u32)lane * 16u, w1k = (u32)w * 1024u, lw16 = l16 + w1k;
#define DMA_RUN(vimg, first, n) sfor<0, n>([&](auto dc) { dma<(first) + 4 * dc.value>(wsd, vimg, w1k); })
  STAMP(0);

  constexpr int SA = 151552, SBB = 40960, SO = SBB + 11520;
  const int ns = tg < 8 ? 3 : 2, s0 = tg < 8 ? 3 * tg : 8 + 2 * tg;
  v2u xres[5];
  const u32 o_pub = WS_O + (u32)(tg * 8 + 2 * w) * 20480u + (u32)(5 * j) * 256u;
  const u32 o_lo = (u32)(tok >> 4) * 20480u + (u32)(tok & 15) * 16u;
  const u32 x_tk = (u32)((w & 1) * 16 + t16), x_lo = ((u32)h * 32u + x_tk) * 16u;
  const u32 x_pub = WS_X + (u32)(tg * 4 + th * 2 + (w >> 1)) * 40960u + (u32)(5 * cg) * 1024u;
  const v4u sv = {SENT, SENT, SENT, SENT};
  u32 oA, oT8, oT16, xA, xT8, xT16, stS, stL, poX, poO, lT, lCS, lV, lK, hkS, hkL, lP, lA, lB, kvS, kvL, lR, lX, lY, lH, lR2, one0;
  u32 E;
  const int wi = 2 * tg + th;
  const u32 fs = w == 3 ? (u32)(R2_OFF + s0 * 1024) : (u32)(s0 * R1_STEP + (w == 1 ? 1024 : 0)), fstep = w == 3 ? 1024u : (u32)R1_STEP;
  auto cs_publish = [&](const v4u (&f)[3], int ty) {
    u32 one2 = 0x3f803f80u;
    asm volatile("" : "+s"(one2));
    float a = 0.f;
#pragma unroll
    for (int i = 0; i < 3; ++i)
#pragma unroll
      for (int c = 0; c < 4; ++c) a = dot2(one2, f[i][c], a);
    a = xor32_add(a);
    const int ln = fresh(lane);
    const u32 idx = (u32)(((ln >> 2) & 1) * 64 + (w == 2 ? 3 : w == 3 ? 2 : w) * 16 + ((ln >> 3) & 3) * 4 + (ln & 3));
    st8((v2u){__float_as_uint(a), E + 1u}, ws, ln < 32 ? idx * 8u : OOB, WS_CS + (u32)((ty * 16 + j) * 16 + tg) * 1024u);
  };
  auto cs_load = [&](v4u (&q)[4], int ty) {
    const u32 o = WS_CS + (u32)((ty * 16 + j) * 16 + 4 * w) * 1024u;
#pragma unroll
    for (int i = 0; i < 4; ++i) ld16_asm<0>(q[i], wsd, l16, o + (u32)i * 1024u);
  };
  auto cs_check = [&](v4u (&q)[4], int ty) -> v2f {
    u32 bad = 0;
#pragma unroll
    for (int i = 0; i < 4; ++i) bad |= (q[i].y ^ (E + 1u)) | (q[i].w ^ (E + 1u));
    if (__builtin_expect(__any(bad != 0u), 0)) {
      COUNT(29);
      for (u32 sp = 0; sp < SPIN_CAP; ++sp) {
        asm volatile("" ::: "memory");
        bad = 0;
#pragma unroll
        for (int i = 0; i < 4; ++i) { q[i] = ld16(ws, l16, WS_CS + (u32)((ty * 16 + j) * 16 + 4 * w + i) * 1024u); bad |= (q[i].y ^ (E + 1u)) | (q[i].w ^ (E + 1u)); }
        if (!__any(bad != 0u)) break;
      }
    }
    return (v2f){(__uint_as_float(q[0].x) + __uint_as_float(q[1].x)) + (__uint_as_float(q[2].x) + __uint_as_float(q[3].x)),
                 (__uint_as_float(q[0].z) + __uint_as_float(q[1].z)) + (__uint_as_float(q[2].z) + __uint_as_float(q[3].z))};
  };
  constexpr int L_CSP = WIMG;
  auto cs_final = [&](int ty) {
    const int ln = fresh(lane);
    const LAS v2f* q = (const LAS v2f*)(L0 + L_CSP) + ln;
    *(LAS v2f*)(L0 + L_CS + ty * 512 + ln * 8) = (q[0] + q[64]) + (q[128] + q[192]);
  };
  {
    const v4i xd = mk_desc(p.x0, 2048 * 640 * 2), dA = mk_desc(p.wqA, 640 * 1920 * 2), dB = mk_desc(p.wqB, 640 * 1920 * 2);
    const v4i doA = mk_desc(p.woA, 640 * 640 * 2), doB = mk_desc(p.woB, 640 * 640 * 2), dp = mk_desc(p.wphi, 32 * 400 * 2);
    const int r4 = lane / 15, cc = lane % 15, part = cc / 5, mem = cc % 5, T = (part + 2) % 3;
    const u32 src = lane < 60 ? (u32)((16 * s0 + 4 * w + r4) * 3840 + part * 1280 + j * 80 + mem * 16) : OOB;
    v4u ca[3], cb[3], co[3], cp;
    u32 ev;
#pragma unroll
    for (int i = 0; i < 3; ++i) in16(ca[i], dA, src, i < ns ? (u32)i * 61440u : OOB);
    asm volatile("buffer_load_dword %0, off, %1, 0 sc1" : "=v"(ev) : "s"(wsd));
    {
      const u64 pc = __builtin_amdgcn_s_getpc() + (u64)min((b >> 3) * 2 + (w & 1), CODE_KB - 1) * 1024u;
      asm volatile("global_load_dwordx4 a[252:255], %0, %1" :: "v"(l16), "s"(pc) : ACC_OWNED);
    }
    const u32 xv = (u32)(128 * tg + 8 * w + (lane >> 3)) * 1280u + (u32)((lane & 7) ^ ((4 * w + G) & 7)) * 16u;
    int blk = j % 10;
    auto x0_run = [&](int n) {
#pragma unroll 1
      for (int i = 0; i < n; ++i) {
        const u32 so = (u32)blk * 128u, ld = (u32)blk * 4096u + w1k;
        dma_x0<0>(xd, xv, so, ld); dma_x0<1>(xd, xv, so, ld); dma_x0<2>(xd, xv, so, ld);
        blk = blk == 9 ? 0 : blk + 1;
      }
    };
    SB();
    x0_run(X0A);
    SB();
    {
      oA = o_lo + (u32)h * 768u; oT8 = o_lo + 512u + (u32)h * 8u; oT16 = h ? OOB : o_lo + 512u;
      xA = x_lo + (u32)(g & 1) * 1024u; xT8 = x_lo + 4096u + (u32)(g & 1) * 8u; xT16 = (g & 1) ? OOB : x_lo + 4096u;
      stS = lane < 16 ? x_tk * 16u : OOB; stL = (u32)h * 2048u + (u32)tok * 16u;
      {
        const int u = lane - 16, v = lane - 48;
        poX = lane < 16 ? (u32)(5 * (lane >> 1) + 4) * 1024u + (u32)(47 + (lane & 1) * 16) * 16u + 8u
            : lane < 48 ? (PREF ? (u32)(64 + (j & 7) * 32 + u) * 128u : OOB)
            : v < 8 ? (u32)(5 * (v >> 2) + 2 * (v & 1) + 1) * 1024u + (u32)(47 + ((v >> 1) & 1) * 16) * 16u + 8u : OOB;
        poO = lane < 16 ? (u32)(5 * lane + 2) * 256u + 248u
            : lane < 48 ? ((PREF && u < 24) ? (u32)(64 + (cg & 3) * 24 + u) * 128u : OOB)
            : v < 14 ? (u32)(5 * (v >> 1) + 3 + (v & 1)) * 256u + 248u : OOB;
      }
      lT = 2048u + (u32)(h * 24 + tok) * 16u; lCS = (u32)(L_CS + h * 256);
      const int rowa = h * 144 + 2 + 32 * w + tok;
      lV = (u32)(L_VP + rowa * 80); lK = (u32)(L_KP + rowa * 48);
      hkS = tok >= 16 ? (u32)(h * 16 + tok - 16) * 16u : OOB; hkL = tg > 0 ? lw16 : OOB;
      { const int hr = (G & 1) * 144 + t16, c = 2 * w + h; lH = (u32)(c < 5 ? L_VP + hr * 80 + c * 16 : L_KP + hr * 48 + (c - 5) * 16); }
      lP = (u32)((h * 128 + 32 * w + tok) * 64 + ((tok >> 1) & 3) * 16);
      const int hd = w >> 1, part = w & 1, mm = rg >> 1, hh = rg & 1;
      const int trow = (hd * 128 + 8 * (G >> 1) + q4) * 64, z0 = q4 >> 1;
      lA = (u32)((part ? L_PB : L_PA) + trow + (((2 * hh + (mm >> 1)) ^ (part ? 3 : 0) ^ z0) * 16) + (mm & 1) * 8);
      lB = (u32)(L_PB + trow + (((rg >> 1) ^ z0) * 16) + (rg & 1) * 8);
      kvS = (tok < 21 && (part == 0 || h == 0)) ? (u32)((hd * 5 + (part ? 4 : 2 * h)) * 21 + tok) * 16u : OOB;
      kvL = (u32)min(tid, 209) * 16u;
      const int t9 = min(64 * w + tok, 209), t8 = min(64 * w + 32 + tok, 209);
      lR = (u32)(L_KVR + (t9 / 21) * 352 + (t9 % 21) * 16 + h * 8); lR2 = (u32)(L_KVR + (t8 / 21) * 352 + (t8 % 21) * 16 + h * 8);
      const int m = tok >> 3, hp = (tok >> 2) & 1, t = tok & 3;
      lX = (u32)(L_KVR + h * 1760 + (hp == h ? 4 * m + t : 21) * 16);
      lY = (u32)(L_KVR + h * 1760 + ((hp == h && (m == 0 || (m == 1 && t == 0))) ? 16 + 4 * m + t : 21) * 16);
      asm volatile("" : "+v"(oA), "+v"(oT8), "+v"(oT16), "+v"(xA), "+v"(xT8), "+v"(xT16), "+v"(stS), "+v"(stL), "+v"(poX), "+v"(poO), "+v"(lT), "+v"(lCS),
                   "+v"(lV), "+v"(lK), "+v"(hkS), "+v"(hkL), "+v"(lP), "+v"(lA), "+v"(lB), "+v"(kvS), "+v"(kvL), "+v"(lR), "+v"(lX), "+v"(lY));
      one0 = tid == 0 ? 0u : OOB;
      asm volatile("" : "+v"(one0), "+v"(lH), "+v"(lR2));
    }
    const u32 stg = (u32)(w * 960 + (mem == 2 ? 512 + r4 * 48 + T * 16 : (T == 2 ? 704 : T * 256) + r4 * 64 + (mem - (mem > 2)) * 16));
    const u32 tbo = (u32)((G >> 1) * 1920 + (rg & 1) * (w == 2 ? 8 : 32) +
                          (w == 2 ? 512 + q4 * 48 + ((rg >> 1) % 3) * 16 : (w == 3 ? 704 : w * 256) + q4 * 64 + (rg >> 1) * 8));
    const u32 fv = w == 2 ? (tok < 24 ? lT : OOB) : l16;
    u32 osrc[3];
#pragma unroll
    for (int i = 0; i < 3; ++i) {
      const int fi = min(w + 4 * i, 9) % 5, bc = 4 * fi + G, bk = bc / 5, cq = bc % 5, i8 = (lane >> 1) & 7;
      osrc[i] = (u32)((32 * wi + 8 * (2 * (bk & 1) + (i8 >> 2)) + 4 * (bk >> 1) + (i8 & 3)) * 1280 + (80 * cg + 16 * cq) * 2 + (lane & 1) * 16);
    }
    u32 xro[5];
#pragma unroll
    for (int c = 0; c < 5; ++c) {
      const int R = 64 * th + 16 * w + t16, cb2 = 160 * cg + 32 * c + 8 * g;
      xro[c] = (u32)((R >> 5) * 40960 + (cb2 >> 7) * 4096 + (R & 31) * 128 + ((((cb2 >> 4) & 7) ^ ((R >> 1) & 7)) * 16) + (cb2 & 8));
    }
    u32 xa = (u32)w * 40960u + (u32)tok * 128u + (u32)(h ^ ((tok >> 1) & 7)) * 16u, f0o = lane < 16 ? flag_off(PH_F0, lane * 16 + j) : OOB;
    u32 stg_ = stg, tbo_ = tbo, fv_ = fv;
    asm volatile("" : "+v"(stg_), "+v"(tbo_), "+v"(fv_), "+v"(osrc[0]), "+v"(osrc[1]), "+v"(osrc[2]), "+v"(xro[0]), "+v"(xro[1]), "+v"(xro[2]), "+v"(xro[3]),
                 "+v"(xro[4]), "+v"(xa), "+v"(f0o));
    STAMP(60);
    asm volatile("s_waitcnt vmcnt(%c4) ; behind a loop" : "+v"(ca[0]), "+v"(ca[1]), "+v"(ca[2]), "+v"(ev) : "i"(3 * X0A + 1));
    if (lane < 60) {
#pragma unroll
      for (int i = 0; i < 3; ++i) *(LAS v4u*)(LP(stg_) + SA + i * 3840) = ca[i];
    }
    E = __builtin_amdgcn_readfirstlane(ev);
    wg_barrier();
    STAMP(61);
    {
      v4u fa[3];
#pragma unroll
      for (int i = 0; i < 3; ++i) fa[i] = __builtin_bit_cast(v4u, trfrag(LP(tbo_) + SA + i * 3840, LP(tbo_) + SA + i * 3840 + 960));
#pragma unroll
      for (int i = 0; i < 3; ++i) st16(fa[i], ws, fv_, i < ns ? WS_WQ + (u32)j * WIMG + fs + (u32)i * fstep : OOB);
      st16(sv, ws, lw16, WS_HK + (u32)cu * 4096u);
      SB();
      cs_publish(fa, 0);
      SB();
    }
    STAMP(62);
    x0_run(X0B - X0A);
    asm volatile("s_waitcnt vmcnt(%c0) ; behind a loop\n\ts_barrier" :: "i"(3 * (X0B - X0A) + 1) : "memory");
    st4(E + 1u, ws, one0, flag_off(PH_F0, cu));
    STAMP(1);
    x0_run(10 - X0B);
    sfor<0, 10>([&](auto ic) { dma_x0<3>(xd, xv, (u32)ic.value * 128u, (u32)ic.value * 4096u + w1k); });
#pragma unroll
    for (int i = 0; i < 3; ++i) in16(co[i], w + 4 * i < 5 ? doA : doB, osrc[i], wi < 20 ? 0u : OOB);
#pragma unroll
    for (int i = 0; i < 3; ++i) in16(cb[i], dB, src, i < ns ? (u32)i * 61440u : OOB);
    in16(cp, dp, tid < 100 ? (u32)(j * 1600 + tid * 16) : OOB, 0u);
    v4u cq4[4];
    flag_put(wsd, f0o);
    cs_load(cq4, 0);
    STAMP(58);
    vmwait<12>();
    asm volatile("s_barrier" ::: "memory");
    STAMP(19);
    {
      const u32 xc[4] = {xa, xa ^ 32u, xa ^ 64u, xa ^ 96u};
      sfor<0, 40>([&](auto ic) { lds_to_acc<AX + 4 * ic.value, (ic.value >> 2) * 4096>(xc[ic.value & 3]); });
#pragma unroll
      for (int c = 0; c < 5; ++c) xres[c] = *(LAS v2u*)LP(xro[c]);
    }
    const v4u st0 = row_sums(xres, E + 1u);
    STAMP(24);
    if (__builtin_expect(!__all(f0o == OOB || flag_get<4>() == E + 1u), 0)) { COUNT(28); poll_flags(ws, f0o, f0o != OOB, E + 1u); }
    asm volatile("" : "+v"(co[0]), "+v"(co[1]), "+v"(co[2]), "+v"(cb[0]), "+v"(cb[1]), "+v"(cb[2]), "+v"(cp));
    wg_barrier();
    STAMP(2);
    DMA_RUN(WS_WQ + (u32)j * WIMG + l16, 0, 9);
    st16(st0, ws, stS, WS_ST + (u32)((tg * 4 + th * 2 + (w >> 1)) * 8 + cg) * 512u);
    SB();
    if (lane < 60) {
#pragma unroll
      for (int i = 0; i < 3; ++i) *(LAS v4u*)(LP(stg_) + SBB + i * 3840) = cb[i];
    }
#pragma unroll
    for (int i = 0; i < 3; ++i) *(LAS v4u*)(L0 + SO + min(w + 4 * i, 9) * 1024 + l16) = co[i];
    if (tid < 100) *(LAS v4u*)(L0 + L_WPHI + tid * 16) = cp;
    if (tid < 10) *(LAS v4u*)(L0 + L_KVR + tid * 352 + 336) = (v4u){0u, 0u, 0u, 0u};
    wait4<10>(cq4);
    *(LAS v2f*)(L0 + L_CSP + w * 512 + lane * 8) = cs_check(cq4, 0);
    wg_barrier();
    cs_final(0);
    {
      const lptr ob = L0 + SO + (G >> 1) * 1280 + ((G & 1) * 4 + q4) * 32 + p4 * 8;
      sfor<0, 3>([&](auto ic) {
        constexpr int i = ic.value;
        const int f = min(w + 4 * i, 9);
        const lptr a = ob + (f / 5) * 5120 + (f % 5) * 256;
        st16(__builtin_bit_cast(v4u, trfrag(a, a + 2560)), ws, l16, wi < 20 ? WS_WO + (u32)((f / 5) * 8 + cg) * 102400u + (u32)wi * 5120u + (u32)(f % 5) * 1024u : OOB);
        tr_to_acc<82 + 4 * i>(LP(tbo_) + SBB + i * 3840, LP(tbo_) + SBB + i * 3840 + 960);
      });
    }
    vmwait<4>();
    wg_barrier();
  }
  STAMP(3);

  auto fw_off = [&](int ln) -> u32 { return ln < 20 ? flag_off(PH_FW, (ln >> 1) * 16 + 2 * cg + (ln & 1)) : OOB; };
  auto pkF = [&](int ph, bool peers) -> u32 {
    const int ln = fresh(lane);
    return ln < 16 ? flag_off(ph, peers ? ln * 16 + j : tg * 16 + ln) : OOB;
  };
  auto flag_check = [&](u32 seen, u32 off) {
    if (__builtin_expect(!__all(off == OOB || seen == E + 1u), 0)) { COUNT(30); poll_flags(ws, off, off != OOB, E + 1u); }
  };
  int tries = 0;
#pragma unroll 1
  for (int L = 0;;) {
    const int ty = L & 1;
    const u32 tagL = E + 1u + (u32)L;
    const u32 imgN = WS_WQ + (u32)((ty ^ 1) * 16 + j) * WIMG;
    const u32 xb = WS_X + (u32)((L * 16 + tg) * 4 + w) * 40960u;
    const u32 sto = WS_ST + (u32)(((L * 16 + tg) * 4 + w) * 8) * 512u;
    const u32 on = o_pub + (u32)(L + 1) * (128u * 20480u), xn = x_pub + (u32)(L + 1) * (64u * 40960u);
    u32 vW;
    const LAS v4f* cs;
    v16f aK, aV, aT, aQ;
    v4u st[4];
    v4f cc[11];
    v2f nmean, rstd;
    u32 stbad;
    float z0, z1, z2, z3;
    auto ln_stats = [&](auto part) {
      constexpr int k = decltype(part)::value;
      if constexpr (k == 0)
        asm volatile("v_add_f32 %0, %4, %5\n\tv_add_f32 %1, %6, %7\n\tv_add_f32 %2, %8, %9\n\tv_add_f32 %3, %10, %11"
                     : "=&v"(z0), "=&v"(z1), "=&v"(z2), "=&v"(z3)
                     : "v"(st[0].x), "v"(st[1].x), "v"(st[2].x), "v"(st[3].x), "v"(st[0].z), "v"(st[1].z), "v"(st[2].z), "v"(st[3].z));
      else if constexpr (k == 1)
        asm volatile("v_add_f32 %0, %0, %1\n\tv_add_f32 %2, %2, %3\n\tv_mov_b32 %1, %0\n\tv_mov_b32 %3, %2" : "+v"(z0), "+v"(z1), "+v"(z2), "+v"(z3));
      else if constexpr (k == 2)
        asm volatile("s_nop 0\n\tv_permlane32_swap_b32 %0, %1\n\tv_permlane32_swap_b32 %2, %3\n\tv_add_f32 %0, %0, %1\n\tv_add_f32 %2, %2, %3"
                     : "+v"(z0), "+v"(z1), "+v"(z2), "+v"(z3));
      else if constexpr (k == 3)
        asm volatile("v_mul_f32 %0, 0xbacccccd, %0\n\tv_mul_f32 %2, 0x3acccccd, %2\n\tv_fma_f32 %1, -%0, %0, %2\n\tv_max_f32 %1, 0, %1"
                     : "+v"(z0), "+v"(z1), "+v"(z2));
      else if constexpr (k == 4) {
        asm volatile("v_add_f32 %0, 0x3727c5ac, %0\n\tv_rsq_f32 %0, %0\n\tv_xor_b32 %1, %2, %3\n\tv_bitop3_b32 %1, %1, %2, %4 bitop3:0xf6"
                     : "+v"(z1), "=&v"(stbad) : "s"(tagL), "v"(st[0].y), "v"(st[0].w));
        nmean = (v2f){z0, z0}; rstd = (v2f){z1, z1};
      } else {
        asm volatile("v_bitop3_b32 %0, %0, %1, %2 bitop3:0xf6\n\tv_bitop3_b32 %0, %0, %1, %3 bitop3:0xf6\n\tv_bitop3_b32 %0, %0, %1, %4 bitop3:0xf6"
                     : "+v"(stbad) : "s"(tagL), "v"(k == 5 ? st[1].y : st[2].w), "v"(k == 5 ? st[1].w : st[3].y), "v"(k == 5 ? st[2].y : st[3].w));
      }
    };
    lptr m0, m1, t0, t1;
    v4u f0[2], f1[2], f2[2];
    auto p1_begin = [&]() {
      vW = adds(l16, WS_WQ + (u32)((L & 1) * 16 + j) * WIMG);
      cs = (const LAS v4f*)LP(adds(lCS, (u32)(L & 1) * 512u));
      m0 = LP(l16); m1 = LP(addc<20 * R1_STEP>(l16)); t0 = LP(lT); t1 = LP(addc<20 * R1_STEP>(lT));
#pragma unroll
      for (int i = 0; i < 2; ++i) { f0[i] = *(LAS v4u*)(m0 + i * R1_STEP); f1[i] = *(LAS v4u*)(m0 + i * R1_STEP + 1024); f2[i] = *(LAS v4u*)(t0 + i * R1_STEP); }
      SB();
    };
    auto p1_step = [&](auto fc, auto ic) {
      constexpr int s = decltype(ic)::value, n = s + 2;
      constexpr bool FIRST = decltype(fc)::value;
      const lptr mb = (n < 20 ? m0 : m1) + (n % 20) * R1_STEP, tb = (n < 20 ? t0 : t1) + (n % 20) * R1_STEP;
      constexpr int QA = FIRST ? (s < 10 ? 36 + 8 * s : s < 19 ? 112 + 4 * (s - 10) : s == 19 ? 146 : -1) : -1;
      constexpr int QB = FIRST ? (s < 9 ? 40 + 8 * s : -1) : ((s <= 19 && (s & 1)) ? (s < 19 ? 112 + 2 * (s - 1) : 146) : -1);
      if constexpr (FIRST && (s == 11 || s == 18 || s == 25)) {
        if constexpr (s == 11) STAMP(4);
        vmwait<(s == 11 ? 16 : s == 18 ? 19 : 14)>();
        asm volatile("s_barrier" ::: "memory");
        if constexpr (s != 18) st4(E + 1u, ws, one0, flag_off(s == 11 ? PH_FW : PH_F1, cu));
        STAMP(s == 11 ? 5 : s == 18 ? 11 : 63);
      }
      if constexpr (QA >= 0) dma_set<QA>(w1k);
      mfma32_xw<s, s == 0, (FIRST ? -1 : p1_wait(s))>(aK, f0[s & 1], f1[s & 1], f2[s & 1]);
      if constexpr (QA >= 0) dma_go(wsd, vW);
      SB();
      if constexpr (n < 40) f0[s & 1] = *(LAS v4u*)mb;
      SB();
      if constexpr (!FIRST && SEG && (s == 8 || s == 16 || s == 24 || s == 32)) { if (L == 2) STAMP(s == 8 ? 33 : s == 16 ? 39 : s == 24 ? 47 : 52); }
      if constexpr (!FIRST) sfor<p1_issued(s), p1_issued(s + 1)>([&](auto fi) { ld_frag<fi.value>(wsd, l16, xb); });
      if constexpr (FIRST) {
        if constexpr (s == 19) {
          const v4u fb[3] = {{acc_read<82>(), acc_read<83>(), acc_read<84>(), acc_read<85>()}, {acc_read<86>(), acc_read<87>(), acc_read<88>(), acc_read<89>()},
                             {acc_read<90>(), acc_read<91>(), acc_read<92>(), acc_read<93>()}};
          cs_publish(fb, 1);
        }
        if constexpr (s >= 20 && s < 23) {
          constexpr int i = s - 20;
          const int ln = fresh(lane), kh = ln >> 5, r8 = 4 * ((ln >> 4) & 1) + (ln & 3);
          const lptr zero = L0 + L_KVR + 336, wb = L0 + L_WPHI + kh * 800 + ((ln >> 2) & 3) * 40;
          const bool okx = (r8 & 1) == kh, oky = okx && r8 < 2;
          const int d0 = kh ? (8 * i + 4) % 20 : 8 * i, d1 = kh ? (8 * i + 8) % 20 : 8 * i + 4;
          tr_to_acc<AWF + 4 * i>(okx ? wb + d0 * 40 + (r8 >> 1) * 8 : zero, (okx && i < 2) ? wb + d1 * 40 + (r8 >> 1) * 8 : zero);
          tr_to_acc<AWF + 12 + 4 * i>(oky ? wb + d0 * 40 + 32 : zero, (oky && i < 2) ? wb + d1 * 40 + 32 : zero);
        }
        if constexpr (s == 24) st16(sv, ws, oA, o_pub);
        if constexpr (s == 26) st16(sv, ws, oA, o_pub + 256u);
        if constexpr (s == 28) st16(sv, ws, oT16, o_pub);
        if constexpr (s == 30) st16(sv, ws, xA, xn);
        if constexpr (s == 32) st16(sv, ws, xA, xn + 2048u);
        if constexpr (s == 34) st16(sv, ws, xT16, xn);
        if constexpr (s == 23) flag_put(wsd, fw_off(fresh(lane)));
        if constexpr (s == 36) st16(sv, ws, lw16, WS_HK + (u32)(256 + cu) * 4096u);
        if constexpr (s == 38 || s == 39) st16(sv, ws, fresh(tid) < 224 ? lw16 : OOB, WS_KV + (u32)(((s - 38) * 16 + j) * 16 + tg) * 3584u);
      }
      if constexpr (s == 36) { wait4<(FIRST ? 8 : (12 - WIN - NX2 > 0 ? 12 - WIN - NX2 : 0))>(st); ln_stats(IC<0>{}); }
      if constexpr (s == 37) ln_stats(IC<2>{});
      if constexpr (s == 38) ln_stats(IC<4>{});
      if constexpr (s == 39) ln_stats(IC<6>{});
      if constexpr (QB >= 0) dma_set<QB>(w1k);
      SB();
      mfma32_x<s, s == 0>(aV, asbf(f1[s & 1]));
      if constexpr (QB >= 0) dma_go(wsd, vW);
      if constexpr (!FIRST && s >= 21 && s <= 27 && (s & 1)) ld16_asm<(s - 21) * 256>(st[(s - 21) / 2], wsd, stL, sto);
      if constexpr (FIRST && s >= 20 && s < 24) ld16_asm<(s - 20) * 512>(st[s - 20], wsd, stL, sto);
      SB();
      if constexpr (n < 40) f1[s & 1] = *(LAS v4u*)(mb + 1024);
      if constexpr (s >= 36 && s < 39) ln_stats(IC<2 * (s - 36) + 1>{});
      if constexpr (s >= 24 && s < 35) cc[s - 24] = cs[s - 24 < 8 ? s - 24 : s - 20];
      SB();
      mfma32_x<s, s == 0>(aT, asbf(f2[s & 1]));
      if constexpr (FIRST && s >= 10 && s < 13)
        acc_store16<82 + 4 * (s - 10)>(wsd, w == 2 ? ((fresh(lane) & 31) < 24 ? lT : OOB) : l16, s - 10 < ns ? imgN + fs + (u32)(s - 10) * fstep : OOB);
      SB();
      if constexpr (n < 40) f2[s & 1] = *(LAS v4u*)tb;
      SB();
    };
    auto p1_end = [&]() {
      asm volatile("s_nop 10" : "+v"(aK), "+v"(aV), "+v"(aT), "+v"(cc[0]), "+v"(cc[1]), "+v"(cc[2]), "+v"(cc[3]), "+v"(cc[4]), "+v"(cc[5]),
                   "+v"(cc[6]), "+v"(cc[7]), "+v"(cc[8]), "+v"(cc[9]), "+v"(cc[10]));
    };
    if (L == 0) {
      p1_begin();
      sfor<0, 40>([&](auto ic) { p1_step(IC<1>{}, ic); });
      p1_end();
    } else {
      p1_begin();
      if (__builtin_expect(tries != 0 || __any(flag_get<WIN>() == SENT), 0)) {
        COUNT(8 + L);
        vmwait<0>();
        if (tries == 2) strict_wait(ws, fresh(l16), xb, 40);
        for (u32 sp = 0; sp < SPIN_CAP; ++sp) {
          flag_put(wsd, poX, xb);
          frag_window(wsd, l16, xb);
          if (!__any(flag_get<WIN>() == SENT)) break;
        }
      }
      STAMP(4 + L * 14 + 0);
      sfor<0, WIN>([&](auto ic) { p1_step(IC<0>{}, ic); });
      asm volatile("s_nop 9" : "+v"(aK));
      if (__builtin_expect((__any(aK[0] != aK[0]) || (FORCE == 1 && w == (j & 3))) && tries < 2, 0)) { COUNT(0 + L); ++tries; continue; }
      sfor<WIN, 40>([&](auto ic) { p1_step(IC<0>{}, ic); });
      p1_end();
      if (__builtin_expect((__any(aK[0] != aK[0]) || (FORCE == 2 && w == (j & 3))) && tries < 2, 0)) { COUNT(0 + L); ++tries; continue; }
      tries = 0;
    }
    STAMP(4 + L * 14 + 2);
    if (__builtin_expect(__any(stbad != 0u) || (FORCE == 4 && w == (j & 3)), 0)) {
      COUNT(24 + L);
      for (u32 sp = 0; sp < SPIN_CAP; ++sp) {
        asm volatile("" ::: "memory");
#pragma unroll
        for (int i = 0; i < 4; ++i) st[i] = ld16(ws, stL, sto + i * 512u);
        sfor<0, 7>(ln_stats);
        if (!__any(stbad != 0u)) break;
      }
    }
#define LN2(acc, i, c4) (pkfma(nmean, ((i) & 1) ? (v2f){(c4).z, (c4).w} : (v2f){(c4).x, (c4).y}, (v2f){(acc)[2 * (i)], (acc)[2 * (i) + 1]}))
    v2f kk[10], vv[10], qt[2];
#pragma unroll
    for (int i = 0; i < 8; ++i) kk[i] = LN2(aK, i, cc[i >> 1]);
#pragma unroll
    for (int i = 0; i < 2; ++i) kk[8 + i] = LN2(aT, i, cc[8]);
#pragma unroll
    for (int i = 0; i < 10; ++i) kk[i] *= rstd;
    SB();
    auto ln_v = [&](auto ic) {
      constexpr int i = decltype(ic)::value;
      if constexpr (i < 8) vv[i] = LN2(aV, i, cc[4 + (i >> 1)]) * rstd;
      else { vv[i] = LN2(aT, i - 6, cc[9]) * rstd; qt[i - 8] = LN2(aT, i - 4, cc[10]); }
    };
    u32 kp[10];
#pragma unroll
    for (int i = 0; i < 10; ++i) kp[i] = pk2(kk[i].x, kk[i].y);
    const u32 mo = L < 3 ? on : OOB, mx2 = L < 2 ? xn + 64u * 40960u : OOB;
    auto mark = [&](auto ic) {
      constexpr int i = decltype(ic)::value;
      if constexpr (i < 3) st16(sv, ws, i < 2 ? oA : oT16, mo + (i == 1 ? 256u : 0u));
      else st16(sv, ws, i < 5 ? xA : xT16, mx2 + (i == 4 ? 2048u : 0u));
    };
    const u32 vO = adds(l16, WS_WO + (u32)(ty * 8 + cg) * 102400u - (u32)L_WO);
    v4f cq[4];
    lptr bq;
    v4u fq[4];
    auto p2_begin = [&]() {
      bq = LP(addc<R2_OFF>(l16));
#pragma unroll
      for (int i = 0; i < 4; ++i) fq[i] = *(LAS v4u*)(bq + i * 1024);
      SB();
    };
    auto p2_step = [&](auto ic) {
      constexpr int s = decltype(ic)::value;
      if constexpr (s % 3 == 0 && s < 36) dma_set<36 + 4 * (s / 3)>(w1k);
      if constexpr (!(s & 1)) mfma32_xw<s, s == 0, -1>(aQ, fq[s & 3], fq[(s + 1) & 3], fq[(s + 1) & 3]);
      else mfma32_x<s, false>(aQ, asbf(fq[s & 3]));
      if constexpr (s % 3 == 0 && s < 36) dma_go(wsd, vO);
      SB();
      if constexpr (s + 4 < 40) fq[s & 3] = *(LAS v4u*)(bq + (s + 4) * 1024);
      if constexpr (s >= 34 && s < 38) cq[s - 34] = cs[8 + s - 34];
    };
    auto pass2 = [&](auto&& hook) {
      p2_begin();
      sfor<0, 40>([&](auto ic) { p2_step(ic); hook(ic); SB(); });
    };
    auto make_q = [&](float qs, u32 (&qp)[10]) {
      asm volatile("s_nop 11" : "+v"(aQ), "+v"(cq[0]), "+v"(cq[1]), "+v"(cq[2]), "+v"(cq[3]));
      v2f qq[10];
#pragma unroll
      for (int i = 0; i < 8; ++i) qq[i] = LN2(aQ, i, cq[i >> 1]);
#pragma unroll
      for (int i = 0; i < 8; ++i) qq[i] *= (v2f){qs, qs};
      qq[8] = qt[0] * (v2f){qs, qs}; qq[9] = qt[1] * (v2f){qs, qs};
      SB();
#pragma unroll
      for (int i = 0; i < 10; ++i) qp[i] = pk2(qq[i].x, qq[i].y);
    };
    v2f oo[10];
    if (ty == 0) {
      mark(IC<0>{});
      SB();
      sfor<0, 10>(ln_v);
      SB();
      if (L == 0) flag_check(acc_read<95>(), fw_off(fresh(lane)));
      if (w == 3 && tg < 15) {
        const u32 hb = WS_HK + (u32)((L >> 1) * 256 + cu) * 4096u;
#pragma unroll
        for (int i = 0; i < 5; ++i)
          st16(__builtin_bit_cast(v4u, __builtin_shufflevector(vv[2 * i], vv[2 * i + 1], 0, 1, 2, 3)), ws, hkS, hb + i * 512u);
        st16((v4u){kp[0], kp[1], kp[2], kp[3]}, ws, hkS, hb + 2560u);
        st16((v4u){kp[4], kp[5], kp[6], kp[7]}, ws, hkS, hb + 3072u);
        st16((v4u){kp[8], kp[9], 0u, 0u}, ws, hkS, hb + 3584u);
      }
      asm volatile("s_barrier" ::: "memory");
      STAMP(4 + L * 14 + 3);
      const u32 hkb = WS_HK + (u32)((L >> 1) * 256 + cu - 16) * 4096u;
      {
        LAS v4f* vp = (LAS v4f*)(LP(lV) + 14 * 80);
        LAS v4u* kq = (LAS v4u*)(LP(lK) + 14 * 48);
        pass2([&](auto ic) {
          constexpr int s = ic.value;
          if constexpr (s % 3 == 1 && s < 15) vp[s / 3] = __builtin_shufflevector(vv[2 * (s / 3)], vv[2 * (s / 3) + 1], 0, 1, 2, 3);
          if constexpr (s == 16) kq[0] = (v4u){kp[0], kp[1], kp[2], kp[3]};
          if constexpr (s == 19) kq[1] = (v4u){kp[4], kp[5], kp[6], kp[7]};
          if constexpr (s == 22) *(LAS v2u*)(kq + 2) = (v2u){kp[8], kp[9]};
          if constexpr (s == 4 || s == 14) mark(IC<(s == 4 ? 1 : 2)>{});
          if constexpr (s == HKPOS - 2) flag_put(wsd, pkF(PH_F1, true));
          if constexpr (s == HKPOS) halo_load(wsd, hkL, hkb);
        });
      }
      STAMP(4 + L * 14 + 4);
      u32 qp[10];
      make_q(rstd.x * (0.22360679774997896f * 1.4426950408889634f), qp);
      {
        vmwait<(35 - HKPOS) / 3>();
        v4u hk = {acc_read<176>(), acc_read<177>(), acc_read<178>(), acc_read<179>()};
        if (__builtin_expect(!__all(max4(hk) != SENT), 0)) {
          COUNT(20 + L);
          for (u32 sp = 0; sp < SPIN_CAP; ++sp) {
            asm volatile("" ::: "memory");
            hk = ld16(ws, hkL, hkb);
            if (__all(max4(hk) != SENT)) break;
          }
        }
        *(LAS v4u*)LP(lH) = hk;
      }
      wg_barrier();
      if (L == 0) { st4(E + 1u, ws, one0, flag_off(PH_F2, cu)); flag_check(acc_read<95>(), pkF(PH_F1, true)); }
      STAMP(4 + L * 14 + 5);
      const lptr kr = LP(lK), vr = LP(lV);
      asm volatile("" ::: "memory");
      v4u k0[8], k1[8];
      v2u k2[8];
      v4f va[3][5];
      float sc[8];
      k0[0] = (v4u){kp[0], kp[1], kp[2], kp[3]}; k1[0] = (v4u){kp[4], kp[5], kp[6], kp[7]}; k2[0] = (v2u){kp[8], kp[9]};
      auto rdk = [&](auto ic, auto pc) {
        constexpr int i = decltype(ic)::value, q = decltype(pc)::value;
        if constexpr (i < 8) {
          if constexpr (q == 0) k0[i] = *(LAS v4u*)(kr + (7 - i) * 96);
          if constexpr (q == 1) k1[i] = *(LAS v4u*)(kr + (7 - i) * 96 + 16);
          if constexpr (q == 2) k2[i] = *(LAS v2u*)(kr + (7 - i) * 96 + 32);
        }
      };
      auto rdv = [&](auto ic, auto cc_) {
        constexpr int i = decltype(ic)::value, c = decltype(cc_)::value;
        if constexpr (i < 8) va[i % 3][c] = *(LAS v4f*)(vr + (7 - i) * 160 + c * 16);
      };
      sfor<0, 3>([&](auto pc) { rdk(IC<1>{}, pc); });
      SB();
      sfor<0, 8>([&](auto ic) {
        constexpr int i = ic.value;
        if constexpr (i > 0) asm volatile("" : "+v"(k0[i]), "+v"(k1[i]), "+v"(k2[i]));
        dma_set<84 + 4 * i>(w1k);
        float a = dot2(qp[0], k0[i].x, 0.f);
        a = dot2(qp[1], k0[i].y, a);
        SB(); rdk(IC<i + 2>{}, IC<0>{}); dma_go(wsd, vO); SB();
        a = dot2(qp[2], k0[i].z, a); a = dot2(qp[3], k0[i].w, a); a = dot2(qp[4], k1[i].x, a);
        SB(); rdk(IC<i + 2>{}, IC<1>{}); if constexpr (i == 0 || i == 3 || i == 6) mark(IC<3 + i / 3>{}); SB();
        a = dot2(qp[5], k1[i].y, a); a = dot2(qp[6], k1[i].z, a); a = dot2(qp[7], k1[i].w, a);
        SB(); rdk(IC<i + 2>{}, IC<2>{}); if constexpr (i >= 3) rdv(IC<1>{}, IC<i - 3>{}); SB();
        a = dot2(qp[8], k2[i].x, a); a = dot2(qp[9], k2[i].y, a);
        asm volatile("" : "+v"(a));
        sc[i] = a;
        if constexpr (i >= 6) { SB(); rdv(IC<2>{}, IC<2 * (i - 6)>{}); rdv(IC<2>{}, IC<2 * (i - 6) + 1>{}); }
        SB();
      });
      if (tg == 0 && w == 0) {
#pragma unroll
        for (int i = 1; i < 8; ++i) if ((fresh(lane) & 31) - 2 * i < 0) sc[i] = -1e9f * 1.4426950408889634f;
      }
      dma_set<116>(w1k);
      const float mx = fmaxf(fmaxf(fmaxf(fmaxf(sc[0], sc[1]), sc[2]), fmaxf(fmaxf(sc[3], sc[4]), sc[5])), fmaxf(sc[6], sc[7]));
      SB(); dma_go(wsd, vO); rdv(IC<2>{}, IC<4>{}); SB();
      sfor<0, 8>([&](auto ic) {
        constexpr int i = ic.value;
        sc[i] = __builtin_amdgcn_exp2f(sc[i] - mx);
        if constexpr (i < 5) { SB(); rdv(IC<3>{}, IC<i>{}); SB(); }
      });
      dma_set<120>(w1k);
      const float rs = __builtin_amdgcn_rcpf(((sc[0] + sc[1]) + (sc[2] + sc[3])) + ((sc[4] + sc[5]) + (sc[6] + sc[7])));
      SB(); dma_go(wsd, vO); SB();
      sfor<0, 8>([&](auto ic) {
        constexpr int i = ic.value;
        const v2f pp = {sc[i], sc[i]};
        if constexpr (i == 0) {
          dma_set<124>(w1k);
#pragma unroll
          for (int e = 0; e < 10; ++e) oo[e] = pp * vv[e];
          SB(); dma_go(wsd, vO); SB();
        } else {
          asm volatile("" : "+v"(va[i % 3][0]), "+v"(va[i % 3][1]), "+v"(va[i % 3][2]), "+v"(va[i % 3][3]), "+v"(va[i % 3][4]));
          if constexpr (i < 3) dma_set<124 + 4 * i>(w1k);
          if constexpr (i >= 2 && i - 2 < NPE) park_load<i - 2>(wsd, lw16, imgN);
          sfor<0, 5>([&](auto cc_) {
            constexpr int c = cc_.value;
            oo[2 * c] = pkfma(pp, va[i % 3][c].xy, oo[2 * c]); oo[2 * c + 1] = pkfma(pp, va[i % 3][c].zw, oo[2 * c + 1]);
            SB(); rdv(IC<i + 3>{}, cc_); if constexpr (i < 3 && c == 0) dma_go(wsd, vO); SB();
          });
        }
      });
#pragma unroll
      for (int e = 0; e < 10; ++e) oo[e] *= (v2f){rs, rs};
    } else {
      asm volatile("s_barrier" ::: "memory");
      {
        v2f pv[10];
        u32 phi[20], vq[10];
        const lptr p0 = LP(lP), p1 = LP(xorc<16>(lP)), p2 = LP(xorc<32>(lP)), p3 = LP(xorc<48>(lP));
        p2_begin();
        feat_proj(kp, pv, [&](auto hc) {
          constexpr int k = hc.value;
          if constexpr (k < 5) { ln_v(IC<2 * k>{}); ln_v(IC<2 * k + 1>{}); }
          if constexpr (k > 0) { vq[2 * k - 2] = pk2(vv[2 * k - 2].x, vv[2 * k - 2].y); vq[2 * k - 1] = pk2(vv[2 * k - 1].x, vv[2 * k - 1].y); }
        });
        dual_softmax(pv, phi, [&](auto hc) {
          constexpr int k = hc.value;
          constexpr int q = k < 3 ? k : (k >= 5 && k < 15) ? k - 2 : (k >= 23 && k < 26) ? k - 10 : -1;
          if constexpr (q >= 0 && q < NQE) { p2_step(IC<q>{}); SB(); }
          if constexpr (k % 4 == 3 && k < 24) mark(IC<k / 4>{});
          if constexpr (k == 7) *(LAS v4u*)(p0 + L_PB) = (v4u){vq[0], vq[1], vq[2], vq[3]};
          if constexpr (k == 10) *(LAS v4u*)(p1 + L_PB) = (v4u){vq[4], vq[5], vq[6], vq[7]};
          if constexpr (k == 13) *(LAS v4u*)(p2 + L_PB) = (v4u){vq[8], vq[9], 0x3f80u, 0u};
          if constexpr (k == 25) *(LAS v4u*)p0 = (v4u){phi[0], phi[1], phi[2], phi[3]};
          if constexpr (k == 26) *(LAS v4u*)p3 = (v4u){phi[12], phi[13], phi[14], phi[15]};
          if constexpr (k == 27) *(LAS v4u*)p1 = (v4u){phi[4], phi[5], phi[6], phi[7]};
        });
        *(LAS v4u*)p2 = (v4u){phi[8], phi[9], phi[10], phi[11]};
        *(LAS v4u*)(p3 + L_PB) = (v4u){phi[16], phi[17], phi[18], phi[19]};
        const lptr a0 = LP(lA), a1 = LP(xorc<32>(lA)) + 256, b0 = LP(lB), b1 = LP(xorc<32>(lB)) + 256;
        wg_barrier();
        v8bf fa[8], fb[8];
        v16f kv;
#pragma unroll
        for (int s = 0; s < 3; ++s) { fa[s] = trfrag(a0 + s * 1024, a1 + s * 1024); fb[s] = trfrag(b0 + s * 1024, b1 + s * 1024); }
        SB();
        sfor<0, 8>([&](auto ic) {
          constexpr int s = ic.value;
          mfma32_p<s == 0>(kv, fa[s], fb[s]);
          SB();
          if constexpr (s + 3 < 8) { fa[s + 3] = trfrag(a0 + (s + 3) * 1024, a1 + (s + 3) * 1024); fb[s + 3] = trfrag(b0 + (s + 3) * 1024, b1 + (s + 3) * 1024); }
          SB();
        });
        MFMA_DONE1(kv);
        const u32 kb = WS_KV + (u32)(((L >> 1) * 16 + j) * 16 + tg) * 3584u;
        st16((v4u){pk2(kv[0], kv[1]), pk2(kv[2], kv[3]), pk2(kv[4], kv[5]), pk2(kv[6], kv[7])}, ws, kvS, kb);
        st16((v4u){pk2(kv[8], kv[9]), pk2(kv[10], kv[11]), pk2(kv[12], kv[13]), pk2(kv[14], kv[15])}, ws, kvS, (w & 1) ? OOB : kb + 336u);
      }
      SB();
      STAMP(4 + L * 14 + 3);
      sfor<NQE, 40>([&](auto ic) {
        p2_step(ic);
        if constexpr (ic.value == 25) flag_put(wsd, pkF(PH_F2, true));
        SB();
      });
      STAMP(4 + L * 14 + 4);
      if (L == 1) flag_check(flag_get<3>(), pkF(PH_F2, true));
      const u32 kvb = WS_KV + (u32)(((L >> 1) * 16 + j) * 16) * 3584u;
      u32 qp[10];
      make_q(rstd.x, qp);
      v2f pv[10];
      u32 phi[20];
      feat_proj(qp, pv, [&](auto hc) { dma<84 + 4 * hc.value>(wsd, vO, w1k); });
      v4u sel;
      dual_softmax(pv, phi, [&](auto kc) {
        constexpr int k = kc.value;
        if constexpr (k < NPE) park_load<k>(wsd, lw16, imgN);
        if constexpr (k == 6) {
          const int d = (fresh(lane) & 31) - 8 * (fresh(lane) >> 5);
#pragma unroll
          for (int c = 0; c < 4; ++c) sel[c] = d == 2 * c ? 0x00003f80u : d == 2 * c + 1 ? 0x3f800000u : 0u;
        }
        if constexpr (k >= KVPOS && k < KVPOS + 8) { kv_load<2 * (k - KVPOS)>(wsd, kvL, kvb); kv_load<2 * (k - KVPOS) + 1>(wsd, kvL, kvb); }
      });
      asm volatile("" : "+v"(phi[0]), "+v"(phi[1]), "+v"(phi[2]), "+v"(phi[3]), "+v"(phi[4]), "+v"(phi[5]), "+v"(phi[6]), "+v"(phi[7]), "+v"(phi[8]), "+v"(phi[9]),
                   "+v"(phi[10]), "+v"(phi[11]), "+v"(phi[12]), "+v"(phi[13]), "+v"(phi[14]), "+v"(phi[15]), "+v"(phi[16]), "+v"(phi[17]), "+v"(phi[18]), "+v"(phi[19]),
                   "+v"(sel));
      SB();
      STAMP(4 + L * 14 + 5);
      {
        v16f sm;
#pragma unroll 1
        for (u32 sp = 0; sp < SPIN_CAP; ++sp) {
          sfor<0, 16>([&](auto ic) { mfma32_xw<ic.value, ic.value == 0, 15 - ic.value>(sm, sel, sel, sel); });
          MFMA_DONE1(sm);
          if (__builtin_expect(!__any(sm[0] != sm[0]) && !(FORCE == 5 && sp == 0 && w == (j & 3)), 1)) break;
          COUNT(16 + L);
          u32 mxs = 0;
          sfor<0, 64>([&](auto ic) { mxs = max(mxs, acc_read<AX + ic.value>()); });
          if (__all(mxs != SENT) && !(FORCE == 5 && sp == 0)) break;
          probe(ws, fresh(lane) < 16 ? kvb + (u32)fresh(lane) * 3584u + 188u * 16u : OOB);
          sfor<0, 16>([&](auto ic) { kv_load<ic.value>(wsd, kvL, kvb); });
        }
        *(LAS v2u*)LP(lR) = (v2u){pk2(sm[0], sm[1]), pk2(sm[2], sm[3])};
        *(LAS v2u*)LP(lR2) = (v2u){pk2(sm[4], sm[5]), pk2(sm[6], sm[7])};
      }
      wg_barrier();
      STAMP(4 + L * 14 + 7);
      const lptr kx = LP(lX), ky = LP(lY);
      v4u ax[5], ay[5];
#pragma unroll
      for (int i = 0; i < 5; ++i) { ay[i] = *(LAS v4u*)(ky + i * 352); ax[i] = *(LAS v4u*)(kx + i * 352); }
      SB();
      v16f X, Y;
      sfor<0, 5>([&](auto ic) {
        constexpr int i = ic.value;
        const v4u bfr = {phi[4 * i], phi[4 * i + 1], phi[4 * i + 2], phi[4 * i + 3]};
        if constexpr (i < 4) dma_set<108 + 8 * i>(w1k);
        mfma32_p<i == 0>(Y, ay[i], bfr);
        if constexpr (i < 4) dma_go(wsd, vO);
        if constexpr (i < 3) dma_set<112 + 8 * i>(w1k);
        mfma32_p<i == 0>(X, ax[i], bfr);
        if constexpr (i < 3) dma_go(wsd, vO);
      });
      MFMA_DONE2(X, Y);
      const float rd = __builtin_amdgcn_rcpf(Y[4] + 1e-6f);
#pragma unroll
      for (int i = 0; i < 8; ++i) oo[i] = (v2f){X[2 * i], X[2 * i + 1]} * (v2f){rd, rd};
      oo[8] = (v2f){Y[0], Y[1]} * (v2f){rd, rd}; oo[9] = (v2f){Y[2], Y[3]} * (v2f){rd, rd};
    }
    SB();
    {
      const u32 ob = o_pub + (u32)L * (128u * 20480u);
      st16((v4u){pk2(oo[0].x, oo[0].y), pk2(oo[1].x, oo[1].y), pk2(oo[2].x, oo[2].y), pk2(oo[3].x, oo[3].y)}, ws, oA, ob);
      st16((v4u){pk2(oo[4].x, oo[4].y), pk2(oo[5].x, oo[5].y), pk2(oo[6].x, oo[6].y), pk2(oo[7].x, oo[7].y)}, ws, oA, ob + 256u);
      st8((v2u){pk2(oo[8].x, oo[8].y), pk2(oo[9].x, oo[9].y)}, ws, oT8, ob);
    }
    SB();
    STAMP(4 + L * 14 + 8);
    if (L == 0) flag_put(wsd, pkF(PH_F2, false));
    sfor<NPE, NPK>([&](auto ic) { park_load<ic.value>(wsd, lw16, imgN); });
    vmwait<3 + NPK - NPE>();
    asm volatile("s_barrier" ::: "memory");
    const lptr w0 = LP(addc<L_WO>(l16)), w1 = LP(addc<L_WO + 10 * 5120>(l16));
    const u32 vN = adds(l16, imgN);
    v2f res[10];
#pragma unroll
    for (int c = 0; c < 5; ++c) { res[2 * c] = (v2f){bflo(xres[c].x), bfhi(xres[c].x)}; res[2 * c + 1] = (v2f){bflo(xres[c].y), bfhi(xres[c].y)}; }
    asm volatile("" : "+v"(res[0]), "+v"(res[1]), "+v"(res[2]), "+v"(res[3]), "+v"(res[4]), "+v"(res[5]), "+v"(res[6]), "+v"(res[7]), "+v"(res[8]), "+v"(res[9]));
    if (L == 0) flag_check(flag_get<NPK - NPE>(), pkF(PH_F2, false));
    const u32 ob = WS_O + (u32)((L * 16 + tg) * 8 + th * 4 + w) * 20480u;
    v4u fa[3][5];
    auto pp_begin = [&]() {
#pragma unroll
      for (int c = 0; c < 5; ++c) fa[0][c] = *(LAS v4u*)(w0 + c * 1024);
#pragma unroll
      for (int c = 0; c < 5; ++c) fa[1][c] = *(LAS v4u*)(w0 + 5120 + c * 1024);
      SB();
    };
    v4f ac[5];
    auto pp_run = [&]() {
      sfor<0, 20>([&](auto ic) {
        constexpr int s = ic.value, n = s + 2;
        const lptr wb = (n < 10 ? w0 : w1) + (n % 10) * 5120;
        sfor<0, 5>([&](auto jc) {
          constexpr int c = jc.value;
          constexpr int q = (s >= NO && ((c == 0 && pp_cnt(s) >= 1) || (c == 3 && pp_cnt(s) == 2))) ? pp_slot(s) + (c == 3) : -1;
          if constexpr (q >= 19 - NPK) dma_set<4 * (q - (19 - NPK))>(w1k);
          if constexpr (c == 0) mfma16_xw<s, s == 0, pp_wait(s)>(ac[0], fa[s % 3]);
          else mfma16_x<s, s == 0>(ac[c], asbf(fa[s % 3][c]));
          if constexpr (c == 0 && s < NO) ld_frag<WIN + s>(wsd, l16, ob);
          if constexpr (q >= 0 && q < 19 - NPK) park_load<NPK + q>(wsd, lw16, imgN);
          if constexpr (q >= 19 - NPK) dma_go(wsd, vN);
          SB();
          if constexpr (n < 20) fa[n % 3][c] = *(LAS v4u*)(wb + c * 1024);
          SB();
        });
      });
      asm volatile("s_nop 7" : "+v"(ac[0]), "+v"(ac[1]), "+v"(ac[2]), "+v"(ac[3]), "+v"(ac[4]));
    };
    pp_begin();
    __builtin_amdgcn_s_sleep(SLP_O);
    STAMP(4 + L * 14 + 9);
    flag_put(wsd, poO, ob);
    frag_window(wsd, l16, ob);
    if (__builtin_expect(__any(flag_get<WIN>() == SENT), 0)) {
      COUNT(12 + L);
      for (u32 sp = 0; sp < SPIN_CAP; ++sp) {
        flag_put(wsd, poO, ob);
        frag_window(wsd, l16, ob);
        if (!__any(flag_get<WIN>() == SENT)) break;
      }
    }
    STAMP(4 + L * 14 + 10);
    pp_run();
    if (__builtin_expect(__any(ac[0][0] != ac[0][0]) || (FORCE == 3 && w == (j & 3)), 0)) {
      COUNT(4 + L);
      strict_wait(ws, fresh(l16), ob, 20);
      frag_window(wsd, l16, ob);
      pp_begin();
      pp_run();
    }
    STAMP(4 + L * 14 + 11);
#pragma unroll
    for (int c = 0; c < 5; ++c) { res[2 * c] += ac[c].xy; res[2 * c + 1] += ac[c].zw; }
    SB();
    v2u xr[5];
#pragma unroll
    for (int c = 0; c < 5; ++c) { xr[c].x = pk2(res[2 * c].x, res[2 * c].y); xr[c].y = pk2(res[2 * c + 1].x, res[2 * c + 1].y); }
    if (L == 3) {
      const int ln = fresh(lane), gg = ln >> 4;
      GAS char* op = (GAS char*)p.out + ((128 * tg + 64 * th + 16 * w + (ln & 15)) * 1280 + 160 * cg);
#pragma unroll
      for (int c = 0; c < 4; c += 2) {
        auto r0 = __builtin_amdgcn_permlane16_swap(xr[c].x, xr[c + 1].x, false, false);
        auto r1 = __builtin_amdgcn_permlane16_swap(xr[c].y, xr[c + 1].y, false, false);
        *(GAS v4u*)(op + 32 * c + (gg & 1) * 32 + (gg >> 1) * 16) = (v4u){r0[0], r1[0], r0[1], r1[1]};
      }
      *(GAS v2u*)(op + 128 + 8 * gg) = xr[4];
      if (b == 0) st4(E + 16u, ws, one0, 0);
      STAMP(59);
      break;
    }
    {
#pragma unroll
      for (int c = 0; c < 4; c += 2) {
        auto r0 = __builtin_amdgcn_permlane16_swap(xr[c].x, xr[c + 1].x, false, false);
        auto r1 = __builtin_amdgcn_permlane16_swap(xr[c].y, xr[c + 1].y, false, false);
        st16((v4u){r0[0], r1[0], r0[1], r1[1]}, ws, xA, xn + (u32)c * 1024u);
      }
      st8(xr[4], ws, xT8, xn);
      SB();
      asm volatile("s_barrier" ::: "memory");
      STAMP(4 + L * 14 + 12);
      const lptr pk0 = LP(addc<36864>(lw16)), pk1 = LP(addc<36864 + 40960>(lw16));
      auto unpark = [&](auto lo, auto hi, auto extra) {
        sfor<decltype(lo)::value, decltype(hi)::value>([&](auto ic) {
          if constexpr (ic.value == NPK) vmwait<12 + decltype(extra)::value>();
          park_store<ic.value>(ic.value < 10 ? pk0 : pk1);
        });
      };
      if (L == 0) {
        v4u cq4[4];
        cs_load(cq4, 1);
        unpark(IC<0>{}, IC<NUP>{}, IC<4>{});
        wait4<0>(cq4);
        *(LAS v2f*)(LP(addc<L_CSP>(fresh(lane) * 8)) + w * 512) = cs_check(cq4, 1);
      } else {
        unpark(IC<0>{}, IC<NUP>{}, IC<0>{});
      }
#pragma unroll
      for (int c = 0; c < 5; ++c) xres[c] = xr[c];
      st16(row_sums(xres, tagL + 1u), ws, stS, WS_ST + (u32)((((L + 1) * 16 + tg) * 4 + th * 2 + (w >> 1)) * 8 + cg) * 512u);
      SB();
      __builtin_amdgcn_s_sleep(SLP_X);
      STAMP(4 + L * 14 + 6);
      flag_put(wsd, poX, xb + 64u * 40960u);
      frag_window(wsd, l16, xb + 64u * 40960u);
      unpark(IC<NUP>{}, IC<19>{}, IC<WIN + 2>{});
      vmwait<5 + WIN>();
      wg_barrier();
      if (L == 0) cs_final(1);
    }
    STAMP(4 + L * 14 + 13);
    ++L;
  }
  DRAIN();
}

extern "C" void kernel_launch(void* const* d_in, const int* in_sizes, int n_in, void* d_out, int out_size, void* d_ws,
                              size_t ws_size, hipStream_t stream) {
  Params p;
  p.x0 = (const u16*)d_in[0]; p.wqA = (const u16*)d_in[1]; p.woA = (const u16*)d_in[2];
  p.wqB = (const u16*)d_in[4]; p.wphi = (const u16*)d_in[5]; p.woB = (const u16*)d_in[6];
  p.out = (u16*)d_out; p.ws = (unsigned char*)d_ws;
  (void)hipFuncSetAttribute((const void*)fused_kernel, hipFuncAttributeMaxDynamicSharedMemorySize, LDS_BYTES);
  fused_kernel<<<256, 256, LDS_BYTES, stream>>>(p);
}
```

```cpp
#include <hip/hip_runtime.h>
#include <hip/hip_bf16.h>

typedef unsigned short u16;
typedef unsigned u32;
typedef unsigned long long u64;
typedef u32 v4u __attribute__((ext_vector_type(4)));
typedef u32 v2u __attribute__((ext_vector_type(2)));
typedef int v4i __attribute__((ext_vector_type(4)));
typedef float v4f __attribute__((ext_vector_type(4)));
typedef float v2f __attribute__((ext_vector_type(2)));
typedef float v16f __attribute__((ext_vector_type(16)));
typedef __bf16 v8bf __attribute__((ext_vector_type(8)));
typedef __bf16 v4bf __attribute__((ext_vector_type(4)));
typedef __bf16 v2bf __attribute__((ext_vector_type(2)));
#define LAS __attribute__((address_space(3)))
#define GAS __attribute__((address_space(1)))
typedef LAS char* lptr;

#define MAP 1
#define SLP_O 10
#define SLP_OB 10
#define KV1 1
#define SLP_X 0
#define KVPOS 6
#define SEG 0
#define NX2 0
#define WINX 8
#define PREF 0
#define NUP 19
#define NQE 13
#define NPE 6
#define NPK 14
#define HKPOS 28
#define SLP_F 0
#define X0A 2
#define X0B 5
constexpr int CODE_KB = 40, CODE_BACK = 0;
#define FORCE 0
#define PROF 0

constexpr u32 WS_FLAG = 4096;
constexpr u32 WS_CS = WS_FLAG + 16 * 256 * 128;
constexpr u32 WS_ST = WS_CS + 2 * 16 * 16 * 1024;
constexpr u32 WS_HK = WS_ST + 4 * 16 * 4 * 8 * 512;
constexpr u32 WS_KV = WS_HK + 2 * 256 * 4096;
constexpr u32 WS_WQ = WS_KV + 2 * 256 * 3584;
constexpr u32 WS_WO = WS_WQ + 2 * 16 * 153600;
constexpr u32 WS_X = WS_WO + 2 * 8 * 102400;
constexpr u32 WS_O = WS_X + 5 * 64 * 40960;
constexpr u32 WS_DBG = WS_O + 5 * 128 * 20480;
constexpr u32 WS_END = WS_DBG + 256 * 4 * 64 * 8 + 4096;
constexpr int R1_STEP = 2816, R2_OFF = 112640, WIMG = 153600;
constexpr int L_CS = 157696;
constexpr int L_KVR = L_CS + 1024;
constexpr int L_WPHI = L_KVR + 3520;
constexpr int L_WO = 36864;
constexpr int L_VP = 0, L_KP = 23040;
constexpr int L_PA = 0, L_PB = 16384;
constexpr int LDS_BYTES = 163840;
constexpr int AUX_SC1 = 16;
constexpr u32 OOB = 0x80000000u;
constexpr u32 SPIN_CAP = 1u << 16;
enum { PH_F0 = 0, PH_F1 = 1, PH_F2 = 2, PH_FW = 3 };

struct Params {
  const u16* x0; const u16* wqA; const u16* woA; const u16* wqB; const u16* wphi; const u16* woB;
  u16* out; unsigned char* ws;
};

typedef __amdgpu_buffer_rsrc_t rsrc_t;
__device__ __forceinline__ rsrc_t mk_rsrc(const void* p, u32 bytes) { return __builtin_amdgcn_make_buffer_rsrc((void*)p, 0, bytes, 0x00020000); }
__device__ __forceinline__ v4u ld16(rsrc_t r, u32 vo, u32 so) { return __builtin_amdgcn_raw_buffer_load_b128(r, vo, so, AUX_SC1); }
__device__ __forceinline__ v2u ld8(rsrc_t r, u32 vo, u32 so) { return __builtin_amdgcn_raw_buffer_load_b64(r, vo, so, AUX_SC1); }
__device__ __forceinline__ u32 ld4(rsrc_t r, u32 vo, u32 so) { return __builtin_amdgcn_raw_buffer_load_b32(r, vo, so, AUX_SC1); }
__device__ __forceinline__ v4u ld16p(rsrc_t r, u32 vo, u32 so) { return __builtin_amdgcn_raw_buffer_load_b128(r, vo, so, 0); }
__device__ __forceinline__ v2u ld8p(rsrc_t r, u32 vo, u32 so) { return __builtin_amdgcn_raw_buffer_load_b64(r, vo, so, 0); }
__device__ __forceinline__ void st16(v4u v, rsrc_t r, u32 vo, u32 so) { __builtin_amdgcn_raw_buffer_store_b128(v, r, vo, so, AUX_SC1); }
__device__ __forceinline__ void st8(v2u v, rsrc_t r, u32 vo, u32 so) { __builtin_amdgcn_raw_buffer_store_b64(v, r, vo, so, AUX_SC1); }
__device__ __forceinline__ void st4(u32 v, rsrc_t r, u32 vo, u32 so) { __builtin_amdgcn_raw_buffer_store_b32(v, r, vo, so, AUX_SC1); }
#define DRAIN() asm volatile("s_waitcnt vmcnt(0)" ::: "memory")
#define VMWAIT(n) asm volatile("s_waitcnt vmcnt(" #n ")" ::: "memory")
#define SB() __builtin_amdgcn_sched_barrier(0)
__device__ __forceinline__ void wg_barrier() {
  asm volatile("s_waitcnt lgkmcnt(0)\n\ts_barrier" ::: "memory");
}
template <int I> struct IC { static constexpr int value = I; };
template <int I, int N, class F> __device__ __forceinline__ void sfor(F&& f) {
  if constexpr (I < N) { f(IC<I>{}); sfor<I + 1, N>(f); }
}
#define A10(p) "a" #p "0", "a" #p "1", "a" #p "2", "a" #p "3", "a" #p "4", "a" #p "5", "a" #p "6", "a" #p "7", "a" #p "8", "a" #p "9"
#define ACC_OWNED "a46", "a47", "a48", "a49", A10(5), A10(6), A10(7), A10(8), "a90", "a91", "a92", "a93", "a94", "a95", "a96", "a97", "a98", "a99", A10(10), A10(11), A10(12), A10(13), A10(14), A10(15), A10(16), A10(17), A10(18), A10(19), \
  A10(20), A10(21), A10(22), A10(23), A10(24), "a250", "a251", "a252", "a253", "a254", "a255"
constexpr int AX = 96;
template <int S> __device__ __forceinline__ void ld_frag(v4i rs, u32 voff, u32 sbase) {
  u32 t;
  asm volatile("s_add_u32 %0, %3, %c6\n\tbuffer_load_dwordx4 a[%c4:%c5], %1, %2, %0 offen sc1"
               : "=&s"(t) : "v"(voff), "s"(rs), "s"(sbase), "i"(AX + 4 * S), "i"(AX + 4 * S + 3), "i"(S * 1024) : "scc", ACC_OWNED);
}
__device__ __forceinline__ void frag_window(v4i rs, u32 voff, u32 sbase) {
  u32 t;
#define FW4(a, b, c, d) "buffer_load_dwordx4 a[" a "], %1, %2, %0 offen sc1\n\tbuffer_load_dwordx4 a[" b "], %1, %2, %0 offen offset:1024 sc1\n\t" \
                        "buffer_load_dwordx4 a[" c "], %1, %2, %0 offen offset:2048 sc1\n\tbuffer_load_dwordx4 a[" d "], %1, %2, %0 offen offset:3072 sc1\n\t"
  asm volatile("s_mov_b32 %0, %3\n\t" FW4("96:99", "100:103", "104:107", "108:111") "s_add_u32 %0, %3, 4096\n\t" FW4("112:115", "116:119", "120:123", "124:127")
               : "=&s"(t) : "v"(voff), "s"(rs), "s"(sbase) : "scc", ACC_OWNED);
}
template <int N> __device__ __forceinline__ void vmwait() { asm volatile("s_waitcnt vmcnt(%c0)" :: "i"(N) : "memory"); }
template <int N> __device__ __forceinline__ void wait3(v4u& a, v4u& b, v4u& c) {
  if constexpr (N >= 0) asm volatile("s_waitcnt vmcnt(%c3)" : "+v"(a), "+v"(b), "+v"(c) : "i"(N));
  else asm volatile("" : "+v"(a), "+v"(b), "+v"(c));
}
template <int N> __device__ __forceinline__ void wait4(v4u (&v)[4]) {
  asm volatile("s_waitcnt vmcnt(%c4)" : "+v"(v[0]), "+v"(v[1]), "+v"(v[2]), "+v"(v[3]) : "i"(N));
}
template <int S, bool FIRST> __device__ __forceinline__ void mfma32_x(v16f& c, v8bf a) {
  if constexpr (FIRST) asm volatile("v_mfma_f32_32x32x16_bf16 %0, %1, a[%c2:%c3], 0" : "=&v"(c) : "v"(a), "i"(AX + 4 * S), "i"(AX + 4 * S + 3));
  else asm volatile("v_mfma_f32_32x32x16_bf16 %0, %1, a[%c2:%c3], %0" : "+v"(c) : "v"(a), "i"(AX + 4 * S), "i"(AX + 4 * S + 3));
}
template <int S, bool FIRST, int N> __device__ __forceinline__ void mfma32_xw(v16f& c, v4u a, v4u o1, v4u o2) {
  if constexpr (FIRST && N >= 0) asm volatile("s_waitcnt vmcnt(%c4)\n\tv_mfma_f32_32x32x16_bf16 %0, %1, a[%c2:%c3], 0" : "=&v"(c) : "v"(a), "i"(AX + 4 * S), "i"(AX + 4 * S + 3), "i"(N), "v"(o1), "v"(o2));
  else if constexpr (N >= 0) asm volatile("s_waitcnt vmcnt(%c4)\n\tv_mfma_f32_32x32x16_bf16 %0, %1, a[%c2:%c3], %0" : "+v"(c) : "v"(a), "i"(AX + 4 * S), "i"(AX + 4 * S + 3), "i"(N), "v"(o1), "v"(o2));
  else if constexpr (FIRST) asm volatile("v_mfma_f32_32x32x16_bf16 %0, %1, a[%c2:%c3], 0" : "=&v"(c) : "v"(a), "i"(AX + 4 * S), "i"(AX + 4 * S + 3), "v"(o1), "v"(o2));
  else asm volatile("v_mfma_f32_32x32x16_bf16 %0, %1, a[%c2:%c3], %0" : "+v"(c) : "v"(a), "i"(AX + 4 * S), "i"(AX + 4 * S + 3), "v"(o1), "v"(o2));
}
template <int S, bool FIRST, int N> __device__ __forceinline__ void mfma16_xw(v4f& c, const v4u (&a)[5]) {
  if constexpr (FIRST) asm volatile("s_waitcnt vmcnt(%c4)\n\tv_mfma_f32_16x16x32_bf16 %0, %1, a[%c2:%c3], 0" : "=&v"(c) : "v"(a[0]), "i"(AX + 4 * S), "i"(AX + 4 * S + 3), "i"(N), "v"(a[1]), "v"(a[2]), "v"(a[3]), "v"(a[4]));
  else asm volatile("s_waitcnt vmcnt(%c4)\n\tv_mfma_f32_16x16x32_bf16 %0, %1, a[%c2:%c3], %0" : "+v"(c) : "v"(a[0]), "i"(AX + 4 * S), "i"(AX + 4 * S + 3), "i"(N), "v"(a[1]), "v"(a[2]), "v"(a[3]), "v"(a[4]));
}
template <int S, bool FIRST> __device__ __forceinline__ void mfma16_x(v4f& c, v8bf a) {
  if constexpr (FIRST) asm volatile("v_mfma_f32_16x16x32_bf16 %0, %1, a[%c2:%c3], 0" : "=&v"(c) : "v"(a), "i"(AX + 4 * S), "i"(AX + 4 * S + 3));
  else asm volatile("v_mfma_f32_16x16x32_bf16 %0, %1, a[%c2:%c3], %0" : "+v"(c) : "v"(a), "i"(AX + 4 * S), "i"(AX + 4 * S + 3));
}
template <bool FIRST, class TA, class TB> __device__ __forceinline__ void mfma32_v(v16f& c, TA a, TB b) {
  if constexpr (FIRST) asm volatile("s_nop 1\n\tv_mfma_f32_32x32x16_bf16 %0, %1, %2, 0" : "=&v"(c) : "v"(a), "v"(b));
  else asm volatile("s_nop 1\n\tv_mfma_f32_32x32x16_bf16 %0, %1, %2, %0" : "+v"(c) : "v"(a), "v"(b));
}
template <bool FIRST, int R> __device__ __forceinline__ void mfma32_av(v16f& c, v4u b) {
  if constexpr (FIRST) asm volatile("s_nop 1\n\tv_mfma_f32_32x32x16_bf16 %0, a[%c2:%c3], %1, 0" : "=&v"(c) : "v"(b), "i"(R), "i"(R + 3));
  else asm volatile("s_nop 1\n\tv_mfma_f32_32x32x16_bf16 %0, a[%c2:%c3], %1, %0" : "+v"(c) : "v"(b), "i"(R), "i"(R + 3));
}
template <bool FIRST, class TA, class TB> __device__ __forceinline__ void mfma32_p(v16f& c, TA a, TB b) {
  if constexpr (FIRST) asm volatile("v_mfma_f32_32x32x16_bf16 %0, %1, %2, 0" : "=&v"(c) : "v"(a), "v"(b));
  else asm volatile("v_mfma_f32_32x32x16_bf16 %0, %1, %2, %0" : "+v"(c) : "v"(a), "v"(b));
}
#define MFMA_DONE1(x) asm volatile("s_nop 11" : "+v"(x))
#define MFMA_DONE2(x, y) asm volatile("s_nop 11" : "+v"(x), "+v"(y))
#define MFMA_DONE3(x, y, z) asm volatile("s_nop 11" : "+v"(x), "+v"(y), "+v"(z))
template <int Q> __device__ __forceinline__ void dma_set(u32 wo) { asm volatile("s_add_u32 m0, %0, %c1" :: "s"(wo), "i"(Q * 1024) : "scc"); }
__device__ __forceinline__ void dma_go(v4i rs, u32 vimg) { asm volatile("buffer_load_dwordx4 %0, %1, m0 offen sc1 lds" :: "v"(vimg), "s"(rs)); }
template <int Q> __device__ __forceinline__ void dma_next(v4i rs, u32 vimg, u32 wo) {
  asm volatile("buffer_load_dwordx4 %0, %1, m0 offen sc1 lds\n\ts_add_u32 m0, %2, %c3" :: "v"(vimg), "s"(rs), "s"(wo), "i"(Q * 1024) : "scc");
}
template <class T> __device__ __forceinline__ void need1(const T& t) { asm volatile("" :: "v"(t)); }
template <class... T> __device__ __forceinline__ void need(const T&... t) { (need1(t), ...); }
template <int Q> __device__ __forceinline__ void dma(v4i rs, u32 vimg, u32 wo) {
  asm volatile("s_add_u32 m0, %2, %c3\n\ts_nop 0\n\tbuffer_load_dwordx4 %0, %1, m0 offen sc1 lds" :: "v"(vimg), "s"(rs), "s"(wo), "i"(Q * 1024) : "scc");
}
template <int TILE> __device__ __forceinline__ void dma_x0(v4i rs, u32 voff, u32 soff, u32 lds) {
  u32 t;
  asm volatile("s_add_u32 m0, %4, %c5\n\ts_add_u32 %0, %3, %c5\n\tbuffer_load_dwordx4 %1, %2, %0 offen lds"
               : "=&s"(t) : "v"(voff), "s"(rs), "s"(soff), "s"(lds), "i"(TILE * 40960) : "scc");
}
__device__ __forceinline__ void in16(v4u& d, v4i rs, u32 voff, u32 soff) { asm volatile("buffer_load_dwordx4 %0, %1, %2, %3 offen" : "=v"(d) : "v"(voff), "s"(rs), "s"(soff)); }
__device__ __forceinline__ void flag_put(v4i rs, u32 voff, u32 soff = 0u) {
  u32 t;
  asm volatile("s_mov_b32 %0, %3\n\tbuffer_load_dword a95, %1, %2, %0 offen sc1" : "=&s"(t) : "v"(voff), "s"(rs), "s"(soff) : ACC_OWNED);
}
template <int N> __device__ __forceinline__ u32 flag_get() {
  u32 v;
  asm volatile("s_waitcnt vmcnt(%c1)\n\tv_accvgpr_read_b32 %0, a95" : "=v"(v) : "i"(N) : "memory");
  return v;
}
__device__ __forceinline__ v4i mk_desc(const void* p, u32 bytes) {
  const u64 a = (u64)p;
  return (v4i){(int)(u32)a, (int)(u32)(a >> 32), (int)bytes, 0x00020000};
}
template <int R, int OFF> __device__ __forceinline__ void lds_to_acc(u32 a) {
  asm volatile("ds_read_b128 a[%c1:%c2], %0 offset:%c3" :: "v"(a), "i"(R), "i"(R + 3), "i"(OFF) : ACC_OWNED);
}
template <int I> __device__ __forceinline__ void park_load(v4i rs, u32 voff, u32 img) {
  u32 t;
  asm volatile("s_add_u32 %0, %3, %c6\n\tbuffer_load_dwordx4 a[%c4:%c5], %1, %2, %0 offen sc1"
               : "=&s"(t) : "v"(voff), "s"(rs), "s"(img), "i"(176 + 4 * I), "i"(179 + 4 * I), "i"((36 + 4 * I) * 1024) : "scc", ACC_OWNED);
}
template <int I> __device__ __forceinline__ void park_store(lptr a) {
  asm volatile("ds_write_b128 %0, a[%c1:%c2] offset:%c3" :: "v"(a), "i"(176 + 4 * I), "i"(179 + 4 * I), "i"((I % 10) * 4096) : "memory");
}
__device__ __forceinline__ void halo_load(v4i rs, u32 voff, u32 base) {
  u32 t;
  asm volatile("s_mov_b32 %0, %3\n\tbuffer_load_dwordx4 a[176:179], %1, %2, %0 offen sc1" : "=&s"(t) : "v"(voff), "s"(rs), "s"(base) : ACC_OWNED);
}
template <int R> __device__ __forceinline__ void tr_to_acc(lptr p0, lptr p1) {
  asm volatile("ds_read_b64_tr_b16 a[%c2:%c3], %0\n\tds_read_b64_tr_b16 a[%c4:%c5], %1" :: "v"(p0), "v"(p1), "i"(R), "i"(R + 1), "i"(R + 2), "i"(R + 3) : "memory", ACC_OWNED);
}
template <int R> __device__ __forceinline__ void acc_store16(v4i rs, u32 voff, u32 soff) {
  u32 t;
  asm volatile("s_mov_b32 %0, %3\n\tbuffer_store_dwordx4 a[%c4:%c5], %1, %2, %0 offen sc1" : "=&s"(t) : "v"(voff), "s"(rs), "s"(soff), "i"(R), "i"(R + 3) : "memory");
}
template <int R> __device__ __forceinline__ u32 acc_read() { u32 v; asm volatile("v_accvgpr_read_b32 %0, a%c1" : "=v"(v) : "i"(R)); return v; }
template <int OFF> __device__ __forceinline__ void ld16_asm(v4u& d, v4i rs, u32 voff, u32 soff) {
  u32 t;
  asm volatile("s_mov_b32 %1, %4\n\tbuffer_load_dwordx4 %0, %2, %3, %1 offen offset:%c5 sc1" : "=v"(d), "=&s"(t) : "v"(voff), "s"(rs), "s"(soff), "i"(OFF));
}
template <int STEP> __device__ __forceinline__ void ld16_step(v4u& d, v4i rs, u32 voff, u32 soff) {
  u32 t;
  asm volatile("s_add_u32 %1, %4, %c5\n\tbuffer_load_dwordx4 %0, %2, %3, %1 offen sc1" : "=v"(d), "=&s"(t) : "v"(voff), "s"(rs), "s"(soff), "i"(STEP) : "scc");
}
constexpr int WIN = WINX;
constexpr int p1_nx(int s) { return s < NX2 ? 2 : s < 40 - WIN - NX2 ? 1 : 0; }
constexpr int p1_issued(int s) { int n = WIN; for (int t = 0; t < s; ++t) n += p1_nx(t); return n; }
constexpr int p1_pre(int s) { return 0; }
constexpr int p1_post(int s) { return (s <= 27 && (s & 1)) ? 1 : 0; }
constexpr int p1_wait(int s) {
  int t0 = -1;
  while (p1_issued(t0 + 1) <= s) ++t0;
  int n = p1_issued(t0 + 1) - 1 - s;
  if (t0 >= 0) n += p1_post(t0) + p1_pre(t0);
  for (int t = t0 + 1; t < s; ++t) n += p1_pre(t) + p1_nx(t) + p1_post(t);
  return n;
}
constexpr int PP_N = 19 - NPK + 9;
constexpr int NO = 20 - WIN;
constexpr int pp_cnt(int s) { return s < NO ? 1 : (PP_N * (s - NO + 1) / WIN - PP_N * (s - NO) / WIN); }
constexpr int pp_slot(int s) { return PP_N * (s - NO) / WIN; }
constexpr int pp_wait(int s) {
  int n = s < WIN ? WIN - 1 - s : 0;
  for (int t = (s < WIN ? 0 : s - WIN + 1); t < s; ++t) n += pp_cnt(t);
  return n;
}
static_assert(PP_N <= 2 * WIN, "at most two pieces a step");
template <int C> __device__ __forceinline__ u32 addc(u32 x) { u32 r; asm volatile("v_add_u32 %0, %c2, %1" : "=v"(r) : "v"(x), "i"(C)); return r; }
template <int C> __device__ __forceinline__ u32 xorc(u32 x) { u32 r; asm volatile("v_xor_b32 %0, %c2, %1" : "=v"(r) : "v"(x), "i"(C)); return r; }
__device__ __forceinline__ u32 adds(u32 x, u32 y) { u32 r; asm volatile("v_add_u32 %0, %2, %1" : "=v"(r) : "v"(x), "s"(y)); return r; }
__device__ __forceinline__ int fresh(int x) { asm volatile("" : "+v"(x)); return x; }
#define LP(x) ((lptr)(size_t)(x))
__device__ __forceinline__ u32 pk2(float a, float b) { v2f f = {a, b}; return __builtin_bit_cast(u32, __builtin_convertvector(f, v2bf)); }
__device__ __forceinline__ float bflo(u32 d) { return __uint_as_float(d << 16); }
__device__ __forceinline__ float bfhi(u32 d) { return __uint_as_float(d & 0xffff0000u); }
__device__ __forceinline__ v8bf asbf(v4u v) { return __builtin_bit_cast(v8bf, v); }
__device__ __forceinline__ v8bf trfrag(lptr p0, lptr p1) {
  v4bf a = __builtin_amdgcn_ds_read_tr16_b64_v4bf16((LAS v4bf*)p0);
  v4bf b = __builtin_amdgcn_ds_read_tr16_b64_v4bf16((LAS v4bf*)p1);
  return __builtin_shufflevector(a, b, 0, 1, 2, 3, 4, 5, 6, 7);
}
__device__ __forceinline__ v16f mfma32(v8bf a, v8bf b, v16f c) { return __builtin_amdgcn_mfma_f32_32x32x16_bf16(a, b, c, 0, 0, 0); }
__device__ __forceinline__ v4f mfma16(v8bf a, v8bf b, v4f c) { return __builtin_amdgcn_mfma_f32_16x16x32_bf16(a, b, c, 0, 0, 0); }
__device__ __forceinline__ float dot2(u32 a, u32 b, float c) {
  return __builtin_amdgcn_fdot2_f32_bf16(__builtin_bit_cast(v2bf, a), __builtin_bit_cast(v2bf, b), c, false);
}
__device__ __forceinline__ void poll_flags(rsrc_t ws, u32 voff, bool on, u32 tag) {
  for (u32 sp = 0; sp < SPIN_CAP; ++sp) {
    u32 v = tag;
    asm volatile("" ::: "memory");
    if (on) v = ld4(ws, voff, 0);
    if (__all(v == tag)) break;
  }
  __builtin_amdgcn_fence(__ATOMIC_ACQUIRE, "wavefront");
}
__device__ __forceinline__ u32 flag_off(int ph, int cu) { return WS_FLAG + (u32)(ph * 256 + cu) * 128u; }

#define COUNT(k) do {} while (0)
#define STAMP(i) do {} while (0)

constexpr u32 SENT = 0xffffffffu;
__device__ __forceinline__ void probe(rsrc_t ws, u32 voff) {
  for (u32 sp = 0; sp < SPIN_CAP; ++sp) {
    asm volatile("" ::: "memory");
    if (__all(ld4(ws, voff, 0) != SENT)) break;
  }
  __builtin_amdgcn_fence(__ATOMIC_ACQUIRE, "wavefront");
}
__device__ __forceinline__ u32 max4(v4u v) { return max(max(v.x, v.y), max(v.z, v.w)); }
__device__ __forceinline__ void strict_wait(rsrc_t ws, u32 voff, u32 base, int n) {
#pragma unroll 1
  for (int s = 0; s < n; s += 10)
    for (u32 sp = 0; sp < SPIN_CAP; ++sp) {
      asm volatile("" ::: "memory");
      v4u v[10];
#pragma unroll
      for (int i = 0; i < 10; ++i) v[i] = ld16(ws, voff, base + (u32)(s + i) * 1024u);
      SB();
      u32 m = 0;
#pragma unroll
      for (int i = 0; i < 10; ++i) m = max(m, max4(v[i]));
      if (__all(m != SENT)) break;
    }
  __builtin_amdgcn_fence(__ATOMIC_ACQUIRE, "wavefront");
}
__device__ __forceinline__ v2f pkfma(v2f a, v2f b, v2f c) { return __builtin_elementwise_fma(a, b, c); }
__device__ __forceinline__ float max3f(float a, float b, float c) { float r; asm("v_max3_f32 %0, %1, %2, %3" : "=v"(r) : "v"(a), "v"(b), "v"(c)); return r; }
__device__ __forceinline__ float min3f(float a, float b, float c) { float r; asm("v_min3_f32 %0, %1, %2, %3" : "=v"(r) : "v"(a), "v"(b), "v"(c)); return r; }
template <class H> __device__ __forceinline__ void dual_softmax(const v2f (&pv)[10], u32 (&phi)[20], H&& hook) {
  const float C = 1.4426950408889634f;
  float mx = max3f(pv[0].x, pv[0].y, pv[0].y), mn = min3f(pv[0].x, pv[0].y, pv[0].y);
  sfor<1, 10>([&](auto ic) {
    constexpr int i = ic.value;
    mx = max3f(mx, pv[i].x, pv[i].y); mn = min3f(mn, pv[i].x, pv[i].y);
    if constexpr (i % 3 == 0) { SB(); hook(IC<i / 3 - 1>{}); SB(); }
  });
  const float a = -mx * C, b = mn * C;
  v2f ep[10], en[10];
  sfor<0, 10>([&](auto ic) {
    constexpr int i = ic.value;
    ep[i] = pkfma(pv[i], (v2f){C, C}, (v2f){a, a}); en[i] = pkfma(pv[i], (v2f){-C, -C}, (v2f){b, b});
    if constexpr (i % 3 == 2) { SB(); hook(IC<3 + i / 3>{}); SB(); }
  });
  SB();
  sfor<0, 10>([&](auto ic) {
    constexpr int i = ic.value;
    ep[i].x = __builtin_amdgcn_exp2f(ep[i].x); ep[i].y = __builtin_amdgcn_exp2f(ep[i].y);
    en[i].x = __builtin_amdgcn_exp2f(en[i].x); en[i].y = __builtin_amdgcn_exp2f(en[i].y);
    SB(); hook(IC<6 + i>{}); SB();
  });
  v2f sp = ep[0], sn = en[0];
  sfor<1, 10>([&](auto ic) {
    constexpr int i = ic.value;
    sp += ep[i]; sn += en[i];
    if constexpr (i % 3 == 0) { SB(); hook(IC<15 + i / 3>{}); SB(); }
  });
  const float rp = __builtin_amdgcn_rcpf(sp.x + sp.y), rn = __builtin_amdgcn_rcpf(sn.x + sn.y);
  sfor<0, 10>([&](auto ic) {
    constexpr int i = ic.value;
    ep[i] *= (v2f){rp, rp}; en[i] *= (v2f){rn, rn};
    if constexpr (i % 2 == 1) { SB(); hook(IC<19 + i / 2>{}); SB(); }
  });
  SB();
  sfor<0, 10>([&](auto ic) {
    constexpr int i = ic.value;
    phi[i] = pk2(ep[i].x, ep[i].y); phi[10 + i] = pk2(en[i].x, en[i].y);
    if constexpr (i % 2 == 1 && i < 9) { SB(); hook(IC<24 + i / 2>{}); SB(); }
  });
}
__device__ __forceinline__ float xor32_add(float v);
__device__ __forceinline__ float xor16_add(float v);
__device__ __forceinline__ v4u row_sums(const v2u (&x)[5], u32 tag) {
  u32 one2 = 0x3f803f80u;
  asm volatile("" : "+s"(one2));
  float s1 = 0.f, s2 = 0.f;
#pragma unroll
  for (int c = 0; c < 5; ++c) { s1 = dot2(one2, x[c].x, s1); s2 = dot2(x[c].x, x[c].x, s2); s1 = dot2(one2, x[c].y, s1); s2 = dot2(x[c].y, x[c].y, s2); }
  s1 = xor32_add(xor16_add(s1)); s2 = xor32_add(xor16_add(s2));
  return (v4u){__float_as_uint(s1), tag, __float_as_uint(s2), tag};
}
__device__ __forceinline__ float xor32_add(float v) {
  auto r = __builtin_amdgcn_permlane32_swap(__float_as_uint(v), __float_as_uint(v), false, false);
  return __uint_as_float(r[0]) + __uint_as_float(r[1]);
}
__device__ __forceinline__ float xor16_add(float v) {
  auto r = __builtin_amdgcn_permlane16_swap(__float_as_uint(v), __float_as_uint(v), false, false);
  return __uint_as_float(r[0]) + __uint_as_float(r[1]);
}
template <int R, int OFF> __device__ __forceinline__ void lds_to_acc1(lptr a) {
  asm volatile("ds_read_b128 a[%c1:%c2], %0 offset:%c3" :: "v"(a), "i"(R), "i"(R + 3), "i"(OFF) : "memory", ACC_OWNED);
}
template <int R> __device__ __forceinline__ void acc_put4(v4u v) {
  asm volatile("v_accvgpr_write_b32 a%c4, %0\n\tv_accvgpr_write_b32 a%c5, %1\n\tv_accvgpr_write_b32 a%c6, %2\n\tv_accvgpr_write_b32 a%c7, %3"
               :: "v"(v.x), "v"(v.y), "v"(v.z), "v"(v.w), "i"(R), "i"(R + 1), "i"(R + 2), "i"(R + 3) : ACC_OWNED);
}
template <int R> __device__ __forceinline__ v4u acc_get4() { return (v4u){acc_read<R>(), acc_read<R + 1>(), acc_read<R + 2>(), acc_read<R + 3>()}; }
template <int I> __device__ __forceinline__ void kv_load(v4i rs, u32 voff, u32 base) {
  u32 t;
  asm volatile("s_add_u32 %0, %3, %c6\n\tbuffer_load_dwordx4 a[%c4:%c5], %1, %2, %0 offen sc1"
               : "=&s"(t) : "v"(voff), "s"(rs), "s"(base), "i"(AX + 4 * I), "i"(AX + 4 * I + 3), "i"(I * 3584) : "scc", ACC_OWNED);
}

__global__ void __launch_bounds__(256, 1) fused_kernel(Params p) {
  extern __shared__ __attribute__((aligned(16))) char smem[];
  const lptr L0 = (lptr)smem;
  const int tid = threadIdx.x, lane = tid & 63, w = __builtin_amdgcn_readfirstlane(tid >> 6);
  const int b = blockIdx.x;
  const int tg = ((b & 7) >> 1) * 4 + (b >> 6), j = (b & 1) * 8 + ((b >> 3) & 7);
  const int cu = tg * 16 + j, th = j & 1, cg = j >> 1;
  const rsrc_t ws = mk_rsrc(p.ws, WS_END);
  const v4i wsd = mk_desc(p.ws, WS_END);
  const int tok = lane & 31, h = lane >> 5;
  const int t16 = lane & 15, g = lane >> 4;
  const int G = lane >> 4, q4 = (lane >> 2) & 3, p4 = lane & 3, rg = 4 * (G & 1) + p4;
  const u32 l16 = (u32)lane * 16u, w1k = (u32)w * 1024u, lw16 = l16 + w1k;
#define DMA_RUN(vimg, first, n) sfor<0, n>([&](auto dc) { dma<(first) + 4 * dc.value>(wsd, vimg, w1k); })
  STAMP(0);

  constexpr int SA = 151552, SBB = 40960, SO = SBB + 11520;
  const int po = (tg & 2) * 4 + (tg >> 2) * 2 + (tg & 1);
  const int ns = 2 + (po < 8), s0 = 2 * po + min(po, 8);
  v2u xres[5];
  const u32 o_pub = WS_O + (u32)(tg * 8 + 2 * w) * 20480u + (u32)(5 * j) * 256u;
  const u32 o_lo = (u32)(tok >> 4) * 20480u + (u32)(tok & 15) * 16u;
  const u32 x_tk = (u32)((w & 1) * 16 + t16), x_lo = ((u32)h * 32u + x_tk) * 16u;
  const u32 x_pub = WS_X + (u32)(tg * 4 + th * 2 + (w >> 1)) * 40960u + (u32)(5 * cg) * 1024u;
  const v4u sv = {SENT, SENT, SENT, SENT};
  u32 oA, oT8, oT16, xA, xT8, xT16, stS, stL, poX, poO, lT, lCS, lV, lK, hkS, hkL, lP, lA, lB, kvS, kvL, lR, lX, lY, lH, lR2, one0;
  u32 E;
  const int wi = 2 * tg + th;
  const u32 fs = w == 3 ? (u32)(R2_OFF + s0 * 1024) : (u32)(s0 * R1_STEP + (w == 1 ? 1024 : 0)), fstep = w == 3 ? 1024u : (u32)R1_STEP;
  auto cs_publish = [&](const v4u (&f)[3], int ty, int T, int rows) {
    u32 one2 = 0x3f803f80u;
    asm volatile("" : "+s"(one2));
    float a = 0.f;
#pragma unroll
    for (int i = 0; i < 3; ++i)
#pragma unroll
      for (int c = 0; c < 4; ++c) a = dot2(one2, f[i][c], a);
    a = xor32_add(a);
    const int ln = fresh(lane);
    const u32 idx = (u32)(((ln >> 2) & 1) * 64 + T * 16 + ((ln >> 3) & 3) * 4 + (ln & 3));
    st8((v2u){__float_as_uint(a), E + 1u}, ws, (ln < 32 && ((rows >> (ln >> 3)) & 1)) ? idx * 8u : OOB, WS_CS + (u32)((ty * 16 + j) * 16 + tg) * 1024u);
  };
  auto cs_load = [&](v4u (&q)[4], int ty) {
    const u32 o = WS_CS + (u32)((ty * 16 + j) * 16 + 4 * w) * 1024u;
#pragma unroll
    for (int i = 0; i < 4; ++i) ld16_asm<0>(q[i], wsd, l16, o + (u32)i * 1024u);
  };
  auto cs_check = [&](v4u (&q)[4], int ty) -> v2f {
    u32 bad = 0;
#pragma unroll
    for (int i = 0; i < 4; ++i) bad |= (q[i].y ^ (E + 1u)) | (q[i].w ^ (E + 1u));
    if (__builtin_expect(__any(bad != 0u), 0)) {
      COUNT(29);
      for (u32 sp = 0; sp < SPIN_CAP; ++sp) {
        asm volatile("" ::: "memory");
        bad = 0;
#pragma unroll
        for (int i = 0; i < 4; ++i) { q[i] = ld16(ws, l16, WS_CS + (u32)((ty * 16 + j) * 16 + 4 * w + i) * 1024u); bad |= (q[i].y ^ (E + 1u)) | (q[i].w ^ (E + 1u)); }
        if (!__any(bad != 0u)) break;
      }
    }
    return (v2f){(__uint_as_float(q[0].x) + __uint_as_float(q[1].x)) + (__uint_as_float(q[2].x) + __uint_as_float(q[3].x)),
                 (__uint_as_float(q[0].z) + __uint_as_float(q[1].z)) + (__uint_as_float(q[2].z) + __uint_as_float(q[3].z))};
  };
  constexpr int L_CSP = WIMG;
  auto cs_final = [&](int ty) {
    const int ln = fresh(lane);
    const LAS v2f* q = (const LAS v2f*)(L0 + L_CSP) + ln;
    *(LAS v2f*)(L0 + L_CS + ty * 512 + ln * 8) = (q[0] + q[64]) + (q[128] + q[192]);
  };
  {
    const v4i xd = mk_desc(p.x0, 2048 * 640 * 2), dA = mk_desc(p.wqA, 640 * 1920 * 2), dB = mk_desc(p.wqB, 640 * 1920 * 2);
    const v4i doA = mk_desc(p.woA, 640 * 640 * 2), doB = mk_desc(p.woB, 640 * 640 * 2), dp = mk_desc(p.wphi, 32 * 400 * 2);
    const int r4 = lane / 15, cc = lane % 15, part = cc / 5, mem = cc % 5, T = (part + 2) % 3;
    const u32 src = lane < 60 ? (u32)((16 * s0 + 4 * w + r4) * 3840 + part * 1280 + j * 80 + mem * 16) : OOB;
    v4u ca[3], cb[3], co[3], cp;
    u32 ev;
#pragma unroll
    for (int i = 0; i < 3; ++i) in16(ca[i], dA, src, i < ns ? (u32)i * 61440u : OOB);
    asm volatile("buffer_load_dword %0, off, %1, 0 sc1" : "=v"(ev) : "s"(wsd));
    {
      const u64 pc = __builtin_amdgcn_s_getpc() + (u64)min((b >> 3) * 2 + (w & 1), CODE_KB - 1) * 1024u;
      asm volatile("global_load_dwordx4 a[252:255], %0, %1" :: "v"(l16), "s"(pc) : ACC_OWNED);
    }
    const u32 xv = (u32)(128 * tg + 8 * w + (lane >> 3)) * 1280u + (u32)((lane & 7) ^ ((4 * w + G) & 7)) * 16u;
    int blk = j % 10;
    auto x0_run = [&](int n) {
#pragma unroll 1
      for (int i = 0; i < n; ++i) {
        const u32 so = (u32)blk * 128u, ld = (u32)blk * 4096u + w1k;
        dma_x0<0>(xd, xv, so, ld); dma_x0<1>(xd, xv, so, ld); dma_x0<2>(xd, xv, so, ld);
        blk = blk == 9 ? 0 : blk + 1;
      }
    };
    SB();
    x0_run(X0A);
    SB();
    {
      oA = o_lo + (u32)h * 768u; oT8 = o_lo + 512u + (u32)h * 8u; oT16 = h ? OOB : o_lo + 512u;
      xA = x_lo + (u32)(g & 1) * 1024u; xT8 = x_lo + 4096u + (u32)(g & 1) * 8u; xT16 = (g & 1) ? OOB : x_lo + 4096u;
      stS = lane < 16 ? x_tk * 16u : OOB; stL = (u32)h * 2048u + (u32)tok * 16u;
      {
        const int u = lane - 16, v = lane - 48;
        poX = lane < 16 ? (u32)(5 * (lane >> 1) + 4) * 1024u + (u32)(47 + (lane & 1) * 16) * 16u + 8u
            : lane < 48 ? (PREF ? (u32)(64 + (j & 7) * 32 + u) * 128u : OOB)
            : v < 8 ? (u32)(5 * (v >> 2) + 2 * (v & 1) + 1) * 1024u + (u32)(47 + ((v >> 1) & 1) * 16) * 16u + 8u : OOB;
        poO = lane < 16 ? (u32)(5 * lane + 2) * 256u + 248u
            : lane < 48 ? ((PREF && u < 24) ? (u32)(64 + (cg & 3) * 24 + u) * 128u : OOB)
            : v < 14 ? (u32)(5 * (v >> 1) + 3 + (v & 1)) * 256u + 248u : OOB;
      }
      lT = 2048u + (u32)(h * 24 + tok) * 16u; lCS = (u32)(L_CS + h * 256);
      const int rowa = h * 144 + 2 + 32 * w + tok;
      lV = (u32)(L_VP + rowa * 80); lK = (u32)(L_KP + rowa * 48);
      hkS = tok >= 16 ? (u32)(h * 16 + tok - 16) * 16u : OOB; hkL = tg > 0 ? lw16 : OOB;
      { const int hr = (G & 1) * 144 + t16, c = 2 * w + h; lH = (u32)(c < 5 ? L_VP + hr * 80 + c * 16 : L_KP + hr * 48 + (c - 5) * 16); }
      lP = (u32)((h * 128 + 32 * w + tok) * 64 + ((tok >> 1) & 3) * 16);
      const int hd = w >> 1, part = w & 1, mm = rg >> 1, hh = rg & 1;
      const int trow = (hd * 128 + 8 * (G >> 1) + q4) * 64, z0 = q4 >> 1;
      lA = (u32)((part ? L_PB : L_PA) + trow + (((2 * hh + (mm >> 1)) ^ (part ? 3 : 0) ^ z0) * 16) + (mm & 1) * 8);
      lB = (u32)(L_PB + trow + (((rg >> 1) ^ z0) * 16) + (rg & 1) * 8);
      kvS = (tok < 21 && (part == 0 || h == 0)) ? (u32)((hd * 5 + (part ? 4 : 2 * h)) * 21 + tok) * 16u : OOB;
      kvL = (u32)min(tid, 209) * 16u;
      const int t9 = min(64 * w + tok, 209), t8 = min(64 * w + 32 + tok, 209);
      lR = (u32)(L_KVR + (t9 / 21) * 352 + (t9 % 21) * 16 + h * 8); lR2 = (u32)(L_KVR + (t8 / 21) * 352 + (t8 % 21) * 16 + h * 8);
      const int m = tok >> 3, hp = (tok >> 2) & 1, t = tok & 3;
      lX = (u32)(L_KVR + h * 1760 + (hp == h ? 4 * m + t : 21) * 16);
      lY = (u32)(L_KVR + h * 1760 + ((hp == h && (m == 0 || (m == 1 && t == 0))) ? 16 + 4 * m + t : 21) * 16);
      asm volatile("" : "+v"(oA), "+v"(oT8), "+v"(oT16), "+v"(xA), "+v"(xT8), "+v"(xT16), "+v"(stS), "+v"(stL), "+v"(poX), "+v"(poO), "+v"(lT), "+v"(lCS),
                   "+v"(lV), "+v"(lK), "+v"(hkS), "+v"(hkL), "+v"(lP), "+v"(lA), "+v"(lB), "+v"(kvS), "+v"(kvL), "+v"(lR), "+v"(lX), "+v"(lY));
      one0 = tid == 0 ? 0u : OOB;
      asm volatile("" : "+v"(one0), "+v"(lH), "+v"(lR2));
    }
    const u32 stg = (u32)(w * 960 + (mem == 2 ? 512 + r4 * 48 + T * 16 : (T == 2 ? 704 : T * 256) + r4 * 64 + (mem - (mem > 2)) * 16));
    const u32 tbo = (u32)((G >> 1) * 1920 + (rg & 1) * (w == 2 ? 8 : 32) +
                          (w == 2 ? 512 + q4 * 48 + ((rg >> 1) % 3) * 16 : (w == 3 ? 704 : w * 256) + q4 * 64 + (rg >> 1) * 8));
    const u32 fv = w == 2 ? (tok < 24 ? lT : OOB) : l16;
    u32 osrc[3];
#pragma unroll
    for (int i = 0; i < 3; ++i) {
      const int fi = min(w + 4 * i, 9) % 5, bc = 4 * fi + G, bk = bc / 5, cq = bc % 5, i8 = (lane >> 1) & 7;
      osrc[i] = (u32)((32 * wi + 8 * (2 * (bk & 1) + (i8 >> 2)) + 4 * (bk >> 1) + (i8 & 3)) * 1280 + (80 * cg + 16 * cq) * 2 + (lane & 1) * 16);
    }
    u32 xro[5];
#pragma unroll
    for (int c = 0; c < 5; ++c) {
      const int R = 64 * th + 16 * w + t16, cb2 = 160 * cg + 32 * c + 8 * g;
      xro[c] = (u32)((R >> 5) * 40960 + (cb2 >> 7) * 4096 + (R & 31) * 128 + ((((cb2 >> 4) & 7) ^ ((R >> 1) & 7)) * 16) + (cb2 & 8));
    }
    u32 xa = (u32)w * 40960u + (u32)tok * 128u + (u32)(h ^ ((tok >> 1) & 7)) * 16u, f0o = lane < 16 ? flag_off(PH_F0, lane * 16 + j) : OOB;
    u32 stg_ = stg, tbo_ = tbo, fv_ = fv;
    asm volatile("" : "+v"(stg_), "+v"(tbo_), "+v"(fv_), "+v"(osrc[0]), "+v"(osrc[1]), "+v"(osrc[2]), "+v"(xro[0]), "+v"(xro[1]), "+v"(xro[2]), "+v"(xro[3]),
                 "+v"(xro[4]), "+v"(xa), "+v"(f0o));
    STAMP(60);
    asm volatile("s_waitcnt vmcnt(%c4) ; behind a loop" : "+v"(ca[0]), "+v"(ca[1]), "+v"(ca[2]), "+v"(ev) : "i"(3 * X0A + 1));
    if (lane < 60) {
#pragma unroll
      for (int i = 0; i < 3; ++i) *(LAS v4u*)(LP(stg_) + SA + i * 3840) = ca[i];
    }
    E = __builtin_amdgcn_readfirstlane(ev);
    wg_barrier();
    STAMP(61);
    {
      v4u fa[3];
#pragma unroll
      for (int i = 0; i < 3; ++i) fa[i] = __builtin_bit_cast(v4u, trfrag(LP(tbo_) + SA + i * 3840, LP(tbo_) + SA + i * 3840 + 960));
#pragma unroll
      for (int i = 0; i < 3; ++i) st16(fa[i], ws, fv_, i < ns ? WS_WQ + (u32)j * WIMG + fs + (u32)i * fstep : OOB);
      st16(sv, ws, lw16, WS_HK + (u32)cu * 4096u);
      SB();
      cs_publish(fa, 0, w == 2 ? 3 : w == 3 ? 2 : w, 15);
      SB();
    }
    STAMP(62);
    x0_run(X0B - X0A);
    asm volatile("s_waitcnt vmcnt(%c0) ; behind a loop\n\ts_barrier" :: "i"(3 * (X0B - X0A) + 1) : "memory");
    st4(E + 1u, ws, one0, flag_off(PH_F0, cu));
    STAMP(1);
    x0_run(10 - X0B);
    sfor<0, 10>([&](auto ic) { dma_x0<3>(xd, xv, (u32)ic.value * 128u, (u32)ic.value * 4096u + w1k); });
#pragma unroll
    for (int i = 0; i < 3; ++i) in16(co[i], w + 4 * i < 5 ? doA : doB, osrc[i], wi < 20 ? 0u : OOB);
#pragma unroll
    for (int i = 0; i < 3; ++i) in16(cb[i], dB, src, i < ns ? (u32)i * 61440u : OOB);
    in16(cp, dp, tid < 100 ? (u32)(j * 1600 + tid * 16) : OOB, 0u);
    v4u cq4[4];
    flag_put(wsd, f0o);
    cs_load(cq4, 0);
    STAMP(58);
    vmwait<12>();
    asm volatile("s_barrier" ::: "memory");
    STAMP(19);
    {
      const u32 xc[4] = {xa, xa ^ 32u, xa ^ 64u, xa ^ 96u};
      sfor<0, 40>([&](auto ic) { lds_to_acc<AX + 4 * ic.value, (ic.value >> 2) * 4096>(xc[ic.value & 3]); });
#pragma unroll
      for (int c = 0; c < 5; ++c) xres[c] = *(LAS v2u*)LP(xro[c]);
    }
    const v4u st0 = row_sums(xres, E + 1u);
    STAMP(24);
    if (__builtin_expect(!__all(f0o == OOB || flag_get<4>() == E + 1u), 0)) { COUNT(28); poll_flags(ws, f0o, f0o != OOB, E + 1u); }
    asm volatile("" : "+v"(co[0]), "+v"(co[1]), "+v"(co[2]), "+v"(cb[0]), "+v"(cb[1]), "+v"(cb[2]), "+v"(cp));
    wg_barrier();
    STAMP(2);
    DMA_RUN(WS_WQ + (u32)j * WIMG + l16, 0, 9);
    st16(st0, ws, stS, WS_ST + (u32)((tg * 4 + th * 2 + (w >> 1)) * 8 + cg) * 512u);
    SB();
    if (lane < 60) {
#pragma unroll
      for (int i = 0; i < 3; ++i) *(LAS v4u*)(LP(stg_) + SBB + i * 3840) = cb[i];
    }
#pragma unroll
    for (int i = 0; i < 3; ++i) *(LAS v4u*)(L0 + SO + min(w + 4 * i, 9) * 1024 + l16) = co[i];
    if (tid < 100) *(LAS v4u*)(L0 + L_WPHI + tid * 16) = cp;
    if (tid < 10) *(LAS v4u*)(L0 + L_KVR + tid * 352 + 336) = (v4u){0u, 0u, 0u, 0u};
    wait4<10>(cq4);
    *(LAS v2f*)(L0 + L_CSP + w * 512 + lane * 8) = cs_check(cq4, 0);
    wg_barrier();
    cs_final(0);
    {
      const lptr ob = L0 + SO + (G >> 1) * 1280 + ((G & 1) * 4 + q4) * 32 + p4 * 8;
      sfor<0, 3>([&](auto ic) {
        constexpr int i = ic.value;
        const int f = min(w + 4 * i, 9);
        const lptr a = ob + (f / 5) * 5120 + (f % 5) * 256;
        st16(__builtin_bit_cast(v4u, trfrag(a, a + 2560)), ws, l16, wi < 20 ? WS_WO + (u32)((f / 5) * 8 + cg) * 102400u + (u32)wi * 5120u + (u32)(f % 5) * 1024u : OOB);
      });
      if (w == 1 || w == 2) {
        sfor<0, 3>([&](auto ic) {
          const lptr a = LP(tbo_) + SBB + ic.value * 3840;
          if (w == 1) tr_to_acc<82 + 4 * ic.value>(a, a + 960); else tr_to_acc<70 + 4 * ic.value>(a, a + 960);
        });
      } else {
        const int kr = 16 * (tok >> 4) + 8 * ((tok >> 2) & 1) + 4 * ((tok >> 3) & 1) + (tok & 3);
        const lptr rm = L0 + SBB + (kr >> 2) * 960 + (w ? 704 : 0) + (kr & 3) * 64 + h * 16, rt = L0 + SBB + (kr >> 2) * 960 + 512 + (kr & 3) * 48 + (w ? 32 : 0);
        lds_to_acc1<46, 0>(rm); lds_to_acc1<50, 32>(rm); lds_to_acc1<54, 0>(rt);
        lds_to_acc1<58, 7680>(rm); lds_to_acc1<62, 7680 + 32>(rm); lds_to_acc1<66, 7680>(rt);
      }
    }
    vmwait<4>();
    wg_barrier();
  }
  STAMP(3);

  auto fw_off = [&](int ln) -> u32 { return ln < 20 ? flag_off(PH_FW, (ln >> 1) * 16 + 2 * cg + (ln & 1)) : OOB; };
  auto pkF = [&](int ph, bool peers) -> u32 {
    const int ln = fresh(lane);
    return ln < 16 ? flag_off(ph, peers ? ln * 16 + j : tg * 16 + ln) : OOB;
  };
  auto flag_check = [&](u32 seen, u32 off, int id) {
    if (__builtin_expect(!__all(off == OOB || seen == E + 1u), 0)) { COUNT(id); poll_flags(ws, off, off != OOB, E + 1u); }
  };
  int tries = 0;
#pragma unroll 1
  for (int L = 0;;) {
    const int ty = L & 1;
    const u32 tagL = E + 1u + (u32)L;
    const u32 imgN = L < 3 ? WS_WQ + (u32)((ty ^ 1) * 16 + j) * WIMG : OOB;
    const u32 xb = WS_X + (u32)((L * 16 + tg) * 4 + w) * 40960u;
    const u32 sto = WS_ST + (u32)(((L * 16 + tg) * 4 + w) * 8) * 512u;
    const u32 on = o_pub + (u32)(L + 1) * (128u * 20480u), xn = x_pub + (u32)(L + 1) * (64u * 40960u);
    u32 vW;
    const LAS v4f* cs;
    v16f aK, aV, aT, aQ;
    v4u st[4];
    v4f cc[11];
    v2f nmean, rstd;
    u32 stbad;
    float z0, z1, z2, z3;
    auto ln_stats = [&](auto part) {
      constexpr int k = decltype(part)::value;
      if constexpr (k == 0)
        asm volatile("v_add_f32 %0, %4, %5\n\tv_add_f32 %1, %6, %7\n\tv_add_f32 %2, %8, %9\n\tv_add_f32 %3, %10, %11"
                     : "=&v"(z0), "=&v"(z1), "=&v"(z2), "=&v"(z3)
                     : "v"(st[0].x), "v"(st[1].x), "v"(st[2].x), "v"(st[3].x), "v"(st[0].z), "v"(st[1].z), "v"(st[2].z), "v"(st[3].z));
      else if constexpr (k == 1)
        asm volatile("v_add_f32 %0, %0, %1\n\tv_add_f32 %2, %2, %3\n\tv_mov_b32 %1, %0\n\tv_mov_b32 %3, %2" : "+v"(z0), "+v"(z1), "+v"(z2), "+v"(z3));
      else if constexpr (k == 2)
        asm volatile("s_nop 0\n\tv_permlane32_swap_b32 %0, %1\n\tv_permlane32_swap_b32 %2, %3\n\tv_add_f32 %0, %0, %1\n\tv_add_f32 %2, %2, %3"
                     : "+v"(z0), "+v"(z1), "+v"(z2), "+v"(z3));
      else if constexpr (k == 3)
        asm volatile("v_mul_f32 %0, 0xbacccccd, %0\n\tv_mul_f32 %2, 0x3acccccd, %2\n\tv_fma_f32 %1, -%0, %0, %2\n\tv_max_f32 %1, 0, %1"
                     : "+v"(z0), "+v"(z1), "+v"(z2));
      else if constexpr (k == 4) {
        asm volatile("v_add_f32 %0, 0x3727c5ac, %0\n\tv_rsq_f32 %0, %0\n\tv_xor_b32 %1, %2, %3\n\tv_bitop3_b32 %1, %1, %2, %4 bitop3:0xf6"
                     : "+v"(z1), "=&v"(stbad) : "s"(tagL), "v"(st[0].y), "v"(st[0].w));
        nmean = (v2f){z0, z0}; rstd = (v2f){z1, z1};
      } else {
        asm volatile("v_bitop3_b32 %0, %0, %1, %2 bitop3:0xf6\n\tv_bitop3_b32 %0, %0, %1, %3 bitop3:0xf6\n\tv_bitop3_b32 %0, %0, %1, %4 bitop3:0xf6"
                     : "+v"(stbad) : "s"(tagL), "v"(k == 5 ? st[1].y : st[2].w), "v"(k == 5 ? st[1].w : st[3].y), "v"(k == 5 ? st[2].y : st[3].w));
      }
    };
    lptr m0, m1, t0, t1;
    v4u f0[2], f1[2], f2[2];
    auto p1_begin = [&]() {
      vW = adds(l16, WS_WQ + (u32)((L & 1) * 16 + j) * WIMG);
      cs = (const LAS v4f*)LP(adds(lCS, (u32)(L & 1) * 512u));
      m0 = LP(l16); m1 = LP(addc<20 * R1_STEP>(l16)); t0 = LP(lT); t1 = LP(addc<20 * R1_STEP>(lT));
#pragma unroll
      for (int i = 0; i < 2; ++i) { f0[i] = *(LAS v4u*)(m0 + i * R1_STEP); f1[i] = *(LAS v4u*)(m0 + i * R1_STEP + 1024); f2[i] = *(LAS v4u*)(t0 + i * R1_STEP); }
      SB();
    };
    auto p1_step = [&](auto fc, auto ic) {
      constexpr int s = decltype(ic)::value, n = s + 2;
      constexpr bool FIRST = decltype(fc)::value;
      const lptr mb = (n < 20 ? m0 : m1) + (n % 20) * R1_STEP, tb = (n < 20 ? t0 : t1) + (n % 20) * R1_STEP;
      constexpr int QA = FIRST ? (s < 10 ? 36 + 8 * s : s < 19 ? 112 + 4 * (s - 10) : s == 19 ? 146 : -1) : -1;
      constexpr int QB = FIRST ? (s < 9 ? 40 + 8 * s : -1) : ((s <= 19 && (s & 1)) ? (s < 19 ? 112 + 2 * (s - 1) : 146) : -1);
      if constexpr (FIRST && (s == 11 || s == 18 || s == 25)) {
        if constexpr (s == 11) STAMP(4);
        vmwait<(s == 11 ? 16 : s == 18 ? 22 : 12)>();
        asm volatile("s_barrier" ::: "memory");
        if constexpr (s != 18) st4(E + 1u, ws, one0, flag_off(s == 11 ? PH_FW : PH_F1, cu));
        STAMP(s == 11 ? 5 : s == 18 ? 11 : 63);
      }
      if constexpr (QA >= 0) dma_set<QA>(w1k);
      mfma32_xw<s, s == 0, (FIRST ? -1 : p1_wait(s))>(aK, f0[s & 1], f1[s & 1], f2[s & 1]);
      if constexpr (QA >= 0) dma_go(wsd, vW);
      SB();
      if constexpr (n < 40) f0[s & 1] = *(LAS v4u*)mb;
      SB();
      if constexpr (!FIRST && SEG && (s == 8 || s == 16 || s == 24 || s == 32)) { if (L == 2) STAMP(s == 8 ? 33 : s == 16 ? 39 : s == 24 ? 47 : 52); }
      if constexpr (!FIRST) sfor<p1_issued(s), p1_issued(s + 1)>([&](auto fi) { ld_frag<fi.value>(wsd, l16, xb); });
      if constexpr (FIRST) {
        if constexpr (s == 2) {
          if (w == 0 || w == 3) {
            const int ln = fresh(lane), kh = ln >> 5, r8 = 4 * ((ln >> 4) & 1) + (ln & 3), hp = r8 & 1, m = r8 >> 1;
            const lptr zero = L0 + L_KVR + 336, wb = L0 + L_WPHI + hp * 800 + ((ln >> 2) & 3) * 40;
            const bool mt = m == (w ? 2 : 0);
            v8bf mp[2][3];
#pragma unroll
            for (int y = 0; y < 2; ++y) {
              const lptr c = wb + (y ? 32 : m * 8);
              const bool on_ = y == 0 || mt;
              mp[y][0] = trfrag((on_ && hp == 0) ? c + (8 * kh) * 40 : zero, (on_ && hp == 0) ? c + (8 * kh + 4) * 40 : zero);
              mp[y][1] = trfrag((on_ && hp == 1) ? c + (4 + 8 * kh) * 40 : zero, (on_ && hp == 1) ? c + (8 + 8 * kh) * 40 : zero);
              mp[y][2] = trfrag((on_ && hp == 0 && kh == 0) ? c + 16 * 40 : zero, (on_ && hp == 1 && kh == 0) ? c : zero);
            }
            sfor<0, 4>([&](auto qc) {
              constexpr int t = qc.value >> 1, y = qc.value & 1;
              v16f d;
              mfma32_av<true, 46 + 12 * t>(d, __builtin_bit_cast(v4u, mp[y][0]));
              mfma32_av<false, 50 + 12 * t>(d, __builtin_bit_cast(v4u, mp[y][1]));
              mfma32_av<false, 54 + 12 * t>(d, __builtin_bit_cast(v4u, mp[y][2]));
              MFMA_DONE1(d);
              acc_put4<(y ? 70 : 82) + 8 * t>((v4u){pk2(d[0], d[1]), pk2(d[2], d[3]), pk2(d[4], d[5]), pk2(d[6], d[7])});
              if constexpr (t == 0) acc_put4<(y ? 70 : 82) + 4>((v4u){pk2(d[8], d[9]), pk2(d[10], d[11]), pk2(d[12], d[13]), pk2(d[14], d[15])});
            });
          }
        }
        if constexpr (s == 19 || s == 21) {
          constexpr int R = s == 19 ? 82 : 70;
          const v4u fb[3] = {acc_get4<R>(), acc_get4<R + 4>(), acc_get4<R + 8>()};
          cs_publish(fb, 1, s == 19 ? (w == 3 ? 2 : w) : 3, s == 19 ? (w == 2 ? 0 : 15) : (w == 0 ? 1 : w == 2 ? 10 : w == 3 ? 4 : 0));
        }
        if constexpr (s == 24) st16(sv, ws, oA, o_pub);
        if constexpr (s == 26) st16(sv, ws, oA, o_pub + 256u);
        if constexpr (s == 28) st16(sv, ws, oT16, o_pub);
        if constexpr (s == 30) st16(sv, ws, xA, xn);
        if constexpr (s == 32) st16(sv, ws, xA, xn + 2048u);
        if constexpr (s == 34) st16(sv, ws, xT16, xn);
        if constexpr (s == 23) flag_put(wsd, fw_off(fresh(lane)));
        if constexpr (s == 36) st16(sv, ws, lw16, WS_HK + (u32)(256 + cu) * 4096u);
        if constexpr (s == 38 || s == 39) st16(sv, ws, fresh(tid) < 224 ? lw16 : OOB, WS_KV + (u32)(((s - 38) * 16 + j) * 16 + tg) * 3584u);
      }
      if constexpr (s == 36) { wait4<(FIRST ? 8 : (12 - WIN - NX2 > 0 ? 12 - WIN - NX2 : 0))>(st); ln_stats(IC<0>{}); }
      if constexpr (s == 37) ln_stats(IC<2>{});
      if constexpr (s == 38) ln_stats(IC<4>{});
      if constexpr (s == 39) ln_stats(IC<6>{});
      if constexpr (QB >= 0) dma_set<QB>(w1k);
      SB();
      mfma32_x<s, s == 0>(aV, asbf(f1[s & 1]));
      if constexpr (QB >= 0) dma_go(wsd, vW);
      if constexpr (!FIRST && s >= 21 && s <= 27 && (s & 1)) ld16_asm<(s - 21) * 256>(st[(s - 21) / 2], wsd, stL, sto);
      if constexpr (FIRST && s >= 20 && s < 24) ld16_asm<(s - 20) * 512>(st[s - 20], wsd, stL, sto);
      SB();
      if constexpr (n < 40) f1[s & 1] = *(LAS v4u*)(mb + 1024);
      if constexpr (s >= 36 && s < 39) ln_stats(IC<2 * (s - 36) + 1>{});
      if constexpr (s >= 24 && s < 35) cc[s - 24] = cs[s - 24 < 8 ? s - 24 : s - 20];
      SB();
      mfma32_x<s, s == 0>(aT, asbf(f2[s & 1]));
      if constexpr (FIRST && s >= 10 && s < 13) acc_store16<82 + 4 * (s - 10)>(wsd, l16, (s - 10 < ns && w != 2) ? imgN + fs + (u32)(s - 10) * fstep : OOB);
      if constexpr (FIRST && s >= 13 && s < 16)
        acc_store16<70 + 4 * (s - 13)>(wsd, ((fresh(lane) >> 3) & 3) == (w == 0 ? 0 : w == 2 ? 1 : 2) ? lT : OOB,
                                       (s - 13 < ns && w != 1) ? imgN + (u32)(s0 + s - 13) * R1_STEP : OOB);
      SB();
      if constexpr (n < 40) f2[s & 1] = *(LAS v4u*)tb;
      SB();
    };
    auto p1_end = [&]() {
      asm volatile("s_nop 10" : "+v"(aK), "+v"(aV), "+v"(aT), "+v"(cc[0]), "+v"(cc[1]), "+v"(cc[2]), "+v"(cc[3]), "+v"(cc[4]), "+v"(cc[5]),
                   "+v"(cc[6]), "+v"(cc[7]), "+v"(cc[8]), "+v"(cc[9]), "+v"(cc[10]));
    };
    if (L == 0) {
      p1_begin();
      sfor<0, 40>([&](auto ic) { p1_step(IC<1>{}, ic); });
      p1_end();
    } else {
      p1_begin();
      if (__builtin_expect(tries != 0 || __any(flag_get<WIN>() == SENT), 0)) {
        COUNT(8 + L);
        vmwait<0>();
        if (tries == 2) strict_wait(ws, fresh(l16), xb, 40);
        for (u32 sp = 0; sp < SPIN_CAP; ++sp) {
          flag_put(wsd, poX, xb);
          frag_window(wsd, l16, xb);
          if (!__any(flag_get<WIN>() == SENT)) break;
        }
      }
      STAMP(4 + L * 14 + 0);
      sfor<0, WIN>([&](auto ic) { p1_step(IC<0>{}, ic); });
      asm volatile("s_nop 9" : "+v"(aK));
      if (__builtin_expect((__any(aK[0] != aK[0]) || (FORCE == 1 && w == (j & 3))) && tries < 2, 0)) { COUNT(0 + L); ++tries; continue; }
      sfor<WIN, 40>([&](auto ic) { p1_step(IC<0>{}, ic); });
      p1_end();
      if (__builtin_expect((__any(aK[0] != aK[0]) || (FORCE == 2 && w == (j & 3))) && tries < 2, 0)) { COUNT(0 + L); ++tries; continue; }
      tries = 0;
    }
    STAMP(4 + L * 14 + 2);
    if (__builtin_expect(__any(stbad != 0u) || (FORCE == 4 && w == (j & 3)), 0)) {
      COUNT(24 + L);
      for (u32 sp = 0; sp < SPIN_CAP; ++sp) {
        asm volatile("" ::: "memory");
#pragma unroll
        for (int i = 0; i < 4; ++i) st[i] = ld16(ws, stL, sto + i * 512u);
        sfor<0, 7>(ln_stats);
        if (!__any(stbad != 0u)) break;
      }
    }
#define LN2(acc, i, c4) (pkfma(nmean, ((i) & 1) ? (v2f){(c4).z, (c4).w} : (v2f){(c4).x, (c4).y}, (v2f){(acc)[2 * (i)], (acc)[2 * (i) + 1]}))
    v2f kk[10], vv[10], qt[2];
#pragma unroll
    for (int i = 0; i < 8; ++i) kk[i] = LN2(aK, i, cc[i >> 1]);
#pragma unroll
    for (int i = 0; i < 2; ++i) kk[8 + i] = LN2(aT, i, cc[8]);
#pragma unroll
    for (int i = 0; i < 10; ++i) kk[i] *= rstd;
    SB();
    auto ln_v = [&](auto ic) {
      constexpr int i = decltype(ic)::value;
      if constexpr (i < 8) vv[i] = LN2(aV, i, cc[4 + (i >> 1)]) * rstd;
      else { vv[i] = LN2(aT, i - 6, cc[9]) * rstd; qt[i - 8] = LN2(aT, i - 4, cc[10]); }
    };
    const u32 mo = L < 3 ? on : OOB, mx2 = L < 2 ? xn + 64u * 40960u : OOB;
    auto mark = [&](auto ic) {
      constexpr int i = decltype(ic)::value;
      if constexpr (i < 3) st16(sv, ws, i < 2 ? oA : oT16, mo + (i == 1 ? 256u : 0u));
      else st16(sv, ws, i < 5 ? xA : xT16, mx2 + (i == 4 ? 2048u : 0u));
    };
    const u32 vO = adds(l16, WS_WO + (u32)(ty * 8 + cg) * 102400u - (u32)L_WO);
    v4f cq[4];
    lptr bq;
    v4u fq[4];
    auto p2_begin = [&]() {
      bq = LP(addc<R2_OFF>(l16));
#pragma unroll
      for (int i = 0; i < 4; ++i) fq[i] = *(LAS v4u*)(bq + i * 1024);
      SB();
    };
    auto p2_step = [&](auto ic) {
      constexpr int s = decltype(ic)::value;
      if constexpr (s % 3 == 0 && s < 36) dma_set<36 + 4 * (s / 3)>(w1k);
      if constexpr (!(s & 1)) mfma32_xw<s, s == 0, -1>(aQ, fq[s & 3], fq[(s + 1) & 3], fq[(s + 1) & 3]);
      else mfma32_x<s, false>(aQ, asbf(fq[s & 3]));
      if constexpr (s % 3 == 0 && s < 36) dma_go(wsd, vO);
      SB();
      if constexpr (s + 4 < 40) fq[s & 3] = *(LAS v4u*)(bq + (s + 4) * 1024);
      if constexpr (s >= 34 && s < 38) cq[s - 34] = cs[8 + s - 34];
    };
    auto pass2 = [&](auto&& hook) {
      p2_begin();
      sfor<0, 40>([&](auto ic) { p2_step(ic); hook(ic); SB(); });
    };
    auto make_q = [&](float qs, v2f (&qq)[10]) {
      asm volatile("s_nop 11" : "+v"(aQ), "+v"(cq[0]), "+v"(cq[1]), "+v"(cq[2]), "+v"(cq[3]));
#pragma unroll
      for (int i = 0; i < 8; ++i) qq[i] = LN2(aQ, i, cq[i >> 1]);
#pragma unroll
      for (int i = 0; i < 8; ++i) qq[i] *= (v2f){qs, qs};
      qq[8] = qt[0] * (v2f){qs, qs}; qq[9] = qt[1] * (v2f){qs, qs};
      SB();
    };
    v2f oo[10];
    if (ty == 0) {
      u32 kp[10];
#pragma unroll
      for (int i = 0; i < 10; ++i) kp[i] = pk2(kk[i].x, kk[i].y);
      mark(IC<0>{});
      SB();
      sfor<0, 10>(ln_v);
      SB();
      if (L == 0) flag_check(acc_read<95>(), fw_off(fresh(lane)), 30);
      if (w == 3 && tg < 15) {
        const u32 hb = WS_HK + (u32)((L >> 1) * 256 + cu) * 4096u;
#pragma unroll
        for (int i = 0; i < 5; ++i)
          st16(__builtin_bit_cast(v4u, __builtin_shufflevector(vv[2 * i], vv[2 * i + 1], 0, 1, 2, 3)), ws, hkS, hb + i * 512u);
        st16((v4u){kp[0], kp[1], kp[2], kp[3]}, ws, hkS, hb + 2560u);
        st16((v4u){kp[4], kp[5], kp[6], kp[7]}, ws, hkS, hb + 3072u);
        st16((v4u){kp[8], kp[9], 0u, 0u}, ws, hkS, hb + 3584u);
      }
      asm volatile("s_barrier" ::: "memory");
      STAMP(4 + L * 14 + 3);
      const u32 hkb = WS_HK + (u32)((L >> 1) * 256 + cu - 16) * 4096u;
      {
        LAS v4f* vp = (LAS v4f*)(LP(lV) + 14 * 80);
        LAS v4u* kq = (LAS v4u*)(LP(lK) + 14 * 48);
        pass2([&](auto ic) {
          constexpr int s = ic.value;
          if constexpr (s % 3 == 1 && s < 15) vp[s / 3] = __builtin_shufflevector(vv[2 * (s / 3)], vv[2 * (s / 3) + 1], 0, 1, 2, 3);
          if constexpr (s == 16) kq[0] = (v4u){kp[0], kp[1], kp[2], kp[3]};
          if constexpr (s == 19) kq[1] = (v4u){kp[4], kp[5], kp[6], kp[7]};
          if constexpr (s == 22) *(LAS v2u*)(kq + 2) = (v2u){kp[8], kp[9]};
          if constexpr (s == 4 || s == 14) mark(IC<(s == 4 ? 1 : 2)>{});
          if constexpr (s == HKPOS) halo_load(wsd, hkL, hkb);
        });
      }
      STAMP(4 + L * 14 + 4);
      u32 qp[10];
      {
        v2f qq[10];
        make_q(rstd.x * (0.22360679774997896f * 1.4426950408889634f), qq);
#pragma unroll
        for (int i = 0; i < 10; ++i) qp[i] = pk2(qq[i].x, qq[i].y);
      }
      need(qp[0], qp[1], qp[2], qp[3], qp[4], qp[5], qp[6], qp[7], qp[8], qp[9]);
      {
        vmwait<(35 - HKPOS) / 3>();
        v4u hk = {acc_read<176>(), acc_read<177>(), acc_read<178>(), acc_read<179>()};
        if (__builtin_expect(!__all(max4(hk) != SENT), 0)) {
          COUNT(20 + L);
          for (u32 sp = 0; sp < SPIN_CAP; ++sp) {
            asm volatile("" ::: "memory");
            hk = ld16(ws, hkL, hkb);
            if (__all(max4(hk) != SENT)) break;
          }
        }
        *(LAS v4u*)LP(lH) = hk;
      }
      wg_barrier();
      if (L == 0) { st4(E + 1u, ws, one0, flag_off(PH_F2, cu)); flag_put(wsd, pkF(PH_F1, true)); }
      STAMP(4 + L * 14 + 5);
      const lptr kr = LP(lK), vr = LP(lV);
      asm volatile("" ::: "memory");
      v4u k0[8], k1[8];
      v2u k2[8];
      v4f va[3][5];
      float sc[8];
      k0[0] = (v4u){kp[0], kp[1], kp[2], kp[3]}; k1[0] = (v4u){kp[4], kp[5], kp[6], kp[7]}; k2[0] = (v2u){kp[8], kp[9]};
      auto rdk = [&](auto ic, auto pc) {
        constexpr int i = decltype(ic)::value, q = decltype(pc)::value;
        if constexpr (i < 8) {
          if constexpr (q == 0) k0[i] = *(LAS v4u*)(kr + (7 - i) * 96);
          if constexpr (q == 1) k1[i] = *(LAS v4u*)(kr + (7 - i) * 96 + 16);
          if constexpr (q == 2) k2[i] = *(LAS v2u*)(kr + (7 - i) * 96 + 32);
        }
      };
      auto rdv = [&](auto ic, auto cc_) {
        constexpr int i = decltype(ic)::value, c = decltype(cc_)::value;
        if constexpr (i < 8) va[i % 3][c] = *(LAS v4f*)(vr + (7 - i) * 160 + c * 16);
      };
      dma_set<84>(w1k);
      sfor<0, 3>([&](auto pc) { rdk(IC<1>{}, pc); });
      SB();
      sfor<0, 8>([&](auto ic) {
        constexpr int i = ic.value;
        if constexpr (i > 0) need(k2[i]);
        float a = dot2(qp[0], k0[i].x, 0.f);
        a = dot2(qp[1], k0[i].y, a);
        SB(); rdk(IC<i + 2>{}, IC<0>{}); dma_next<88 + 4 * i>(wsd, vO, w1k); SB();
        a = dot2(qp[2], k0[i].z, a); a = dot2(qp[3], k0[i].w, a); a = dot2(qp[4], k1[i].x, a);
        SB(); rdk(IC<i + 2>{}, IC<1>{}); if constexpr (i == 0 || i == 3 || i == 6) mark(IC<3 + i / 3>{}); SB();
        a = dot2(qp[5], k1[i].y, a); a = dot2(qp[6], k1[i].z, a); a = dot2(qp[7], k1[i].w, a);
        SB(); rdk(IC<i + 2>{}, IC<2>{}); if constexpr (i >= 3) rdv(IC<1>{}, IC<i - 3>{}); SB();
        a = dot2(qp[8], k2[i].x, a); a = dot2(qp[9], k2[i].y, a);
        need(a);
        sc[i] = a;
        if constexpr (i >= 6) { SB(); rdv(IC<2>{}, IC<2 * (i - 6)>{}); rdv(IC<2>{}, IC<2 * (i - 6) + 1>{}); }
        SB();
      });
      if (tg == 0 && w == 0) {
#pragma unroll
        for (int i = 1; i < 8; ++i) if ((fresh(lane) & 31) - 2 * i < 0) sc[i] = -1e9f * 1.4426950408889634f;
      }
      const float mx = fmaxf(fmaxf(fmaxf(fmaxf(sc[0], sc[1]), sc[2]), fmaxf(fmaxf(sc[3], sc[4]), sc[5])), fmaxf(sc[6], sc[7]));
      need(mx); SB(); dma_next<120>(wsd, vO, w1k); rdv(IC<2>{}, IC<4>{}); SB();
      sfor<0, 8>([&](auto ic) {
        constexpr int i = ic.value;
        sc[i] = __builtin_amdgcn_exp2f(sc[i] - mx);
        if constexpr (i < 5) { SB(); rdv(IC<3>{}, IC<i>{}); SB(); }
      });
      const float rs = __builtin_amdgcn_rcpf(((sc[0] + sc[1]) + (sc[2] + sc[3])) + ((sc[4] + sc[5]) + (sc[6] + sc[7])));
      need(rs); SB(); dma_next<124>(wsd, vO, w1k); SB();
#pragma unroll
      for (int i = 0; i < 8; ++i) sc[i] *= rs;
      sfor<0, 8>([&](auto ic) {
        constexpr int i = ic.value;
        const v2f pp = {sc[i], sc[i]};
        if constexpr (i == 0) {
#pragma unroll
          for (int e = 0; e < 10; ++e) oo[e] = pp * vv[e];
          need(oo[9]); SB(); dma_next<128>(wsd, vO, w1k); SB();
        } else {
          need(va[i % 3][4]);
          if constexpr (i >= 2 && i - 2 < NPE) park_load<i - 2>(wsd, lw16, imgN);
          sfor<0, 5>([&](auto cc_) {
            constexpr int c = cc_.value;
            oo[2 * c] = pkfma(pp, va[i % 3][c].xy, oo[2 * c]); oo[2 * c + 1] = pkfma(pp, va[i % 3][c].zw, oo[2 * c + 1]);
            SB(); rdv(IC<i + 3>{}, cc_); if constexpr (i < 3 && c == 2) dma_next<128 + 4 * i>(wsd, vO, w1k); SB();
          });
        }
      });
    } else {
      asm volatile("s_barrier" ::: "memory");
      {
        u32 phi[20], vq[10];
        const lptr p0 = LP(lP), p1 = LP(xorc<16>(lP)), p2 = LP(xorc<32>(lP)), p3 = LP(xorc<48>(lP));
        p2_begin();
        dual_softmax(kk, phi, [&](auto hc) {
          constexpr int k = hc.value;
          if constexpr (k == 3) sfor<0, 10>(ln_v);
          if constexpr (k == 6) {
#pragma unroll
            for (int i = 0; i < 10; ++i) vq[i] = pk2(vv[i].x, vv[i].y);
          }
          constexpr int q = k < 3 ? k : (k >= 5 && k < 15) ? k - 2 : (k >= 23 && k < 26) ? k - 10 : -1;
          if constexpr (q >= 0 && q < NQE) { p2_step(IC<q>{}); SB(); }
          if constexpr (k % 4 == 3 && k < 24) mark(IC<k / 4>{});
          if constexpr (k == 7) *(LAS v4u*)(p0 + L_PB) = (v4u){vq[0], vq[1], vq[2], vq[3]};
          if constexpr (k == 10) *(LAS v4u*)(p1 + L_PB) = (v4u){vq[4], vq[5], vq[6], vq[7]};
          if constexpr (k == 13) *(LAS v4u*)(p2 + L_PB) = (v4u){vq[8], vq[9], 0x3f80u, 0u};
          if constexpr (k == 25) *(LAS v4u*)p0 = (v4u){phi[0], phi[1], phi[2], phi[3]};
          if constexpr (k == 26) *(LAS v4u*)p3 = (v4u){phi[12], phi[13], phi[14], phi[15]};
          if constexpr (k == 27) *(LAS v4u*)p1 = (v4u){phi[4], phi[5], phi[6], phi[7]};
        });
        *(LAS v4u*)p2 = (v4u){phi[8], phi[9], phi[10], phi[11]};
        *(LAS v4u*)(p3 + L_PB) = (v4u){phi[16], phi[17], phi[18], phi[19]};
        const lptr a0 = LP(lA), a1 = LP(xorc<32>(lA)) + 256, b0 = LP(lB), b1 = LP(xorc<32>(lB)) + 256;
        wg_barrier();
        v8bf fa[8], fb[8];
        v16f kv;
#pragma unroll
        for (int s = 0; s < 3; ++s) { fa[s] = trfrag(a0 + s * 1024, a1 + s * 1024); fb[s] = trfrag(b0 + s * 1024, b1 + s * 1024); }
        SB();
        sfor<0, 8>([&](auto ic) {
          constexpr int s = ic.value;
          mfma32_p<s == 0>(kv, fa[s], fb[s]);
          SB();
          if constexpr (s + 3 < 8) { fa[s + 3] = trfrag(a0 + (s + 3) * 1024, a1 + (s + 3) * 1024); fb[s + 3] = trfrag(b0 + (s + 3) * 1024, b1 + (s + 3) * 1024); }
          SB();
        });
        MFMA_DONE1(kv);
        const u32 kb = WS_KV + (u32)(((L >> 1) * 16 + j) * 16 + tg) * 3584u;
        st16((v4u){pk2(kv[0], kv[1]), pk2(kv[2], kv[3]), pk2(kv[4], kv[5]), pk2(kv[6], kv[7])}, ws, kvS, kb);
        st16((v4u){pk2(kv[8], kv[9]), pk2(kv[10], kv[11]), pk2(kv[12], kv[13]), pk2(kv[14], kv[15])}, ws, kvS, (w & 1) ? OOB : kb + 336u);
      }
      SB();
      STAMP(4 + L * 14 + 3);
      sfor<NQE, 40>([&](auto ic) {
        p2_step(ic);
        if constexpr (ic.value == 25) flag_put(wsd, pkF(PH_F2, true));
        SB();
      });
      STAMP(4 + L * 14 + 4);
      if (L == 1) flag_check(flag_get<3>(), pkF(PH_F2, true), 29);
      const u32 kvb = WS_KV + (u32)(((L >> 1) * 16 + j) * 16) * 3584u;
      v2f pv[10];
      make_q(rstd.x, pv);
      u32 phi[20];
      dma_set<84>(w1k);
      v4u sel;
      dual_softmax(pv, phi, [&](auto kc) {
        constexpr int k = kc.value;
        if constexpr (k < 6) dma_next<88 + 4 * k>(wsd, vO, w1k);
        if constexpr (k < NPE) park_load<k>(wsd, lw16, imgN);
        if constexpr (k == 6) {
          const int d = (fresh(lane) & 31) - 8 * (fresh(lane) >> 5);
#pragma unroll
          for (int c = 0; c < 4; ++c) sel[c] = d == 2 * c ? 0x00003f80u : d == 2 * c + 1 ? 0x3f800000u : 0u;
        }
        if constexpr (KV1 && k >= KVPOS && k < KVPOS + 16) kv_load<k - KVPOS>(wsd, kvL, kvb);
        if constexpr (!KV1 && k >= KVPOS && k < KVPOS + 8) { kv_load<2 * (k - KVPOS)>(wsd, kvL, kvb); kv_load<2 * (k - KVPOS) + 1>(wsd, kvL, kvb); }
      });
      asm volatile("" : "+v"(phi[0]), "+v"(phi[1]), "+v"(phi[2]), "+v"(phi[3]), "+v"(phi[4]), "+v"(phi[5]), "+v"(phi[6]), "+v"(phi[7]), "+v"(phi[8]), "+v"(phi[9]),
                   "+v"(phi[10]), "+v"(phi[11]), "+v"(phi[12]), "+v"(phi[13]), "+v"(phi[14]), "+v"(phi[15]), "+v"(phi[16]), "+v"(phi[17]), "+v"(phi[18]), "+v"(phi[19]),
                   "+v"(sel));
      SB();
      STAMP(4 + L * 14 + 5);
      {
        v16f sm;
#pragma unroll 1
        for (u32 sp = 0; sp < SPIN_CAP; ++sp) {
          sfor<0, 16>([&](auto ic) { mfma32_xw<ic.value, ic.value == 0, 15 - ic.value>(sm, sel, sel, sel); });
          MFMA_DONE1(sm);
          if (__builtin_expect(!__any(sm[0] != sm[0]) && !(FORCE == 5 && sp == 0 && w == (j & 3)), 1)) break;
          COUNT(16 + L);
          u32 mxs = 0;
          sfor<0, 64>([&](auto ic) { mxs = max(mxs, acc_read<AX + ic.value>()); });
          if (__all(mxs != SENT) && !(FORCE == 5 && sp == 0)) break;
          probe(ws, fresh(lane) < 16 ? kvb + (u32)fresh(lane) * 3584u + 188u * 16u : OOB);
          sfor<0, 16>([&](auto ic) { kv_load<ic.value>(wsd, kvL, kvb); });
        }
        *(LAS v2u*)LP(lR) = (v2u){pk2(sm[0], sm[1]), pk2(sm[2], sm[3])};
        *(LAS v2u*)LP(lR2) = (v2u){pk2(sm[4], sm[5]), pk2(sm[6], sm[7])};
      }
      wg_barrier();
      STAMP(4 + L * 14 + 7);
      const lptr kx = LP(lX), ky = LP(lY);
      v4u ax[5], ay[5];
#pragma unroll
      for (int i = 0; i < 5; ++i) { ay[i] = *(LAS v4u*)(ky + i * 352); ax[i] = *(LAS v4u*)(kx + i * 352); }
      SB();
      v16f X, Y;
      sfor<0, 5>([&](auto ic) {
        constexpr int i = ic.value;
        const v4u bfr = {phi[4 * i], phi[4 * i + 1], phi[4 * i + 2], phi[4 * i + 3]};
        if constexpr (i < 4) dma_set<108 + 8 * i>(w1k);
        mfma32_p<i == 0>(Y, ay[i], bfr);
        if constexpr (i < 4) dma_go(wsd, vO);
        if constexpr (i < 3) dma_set<112 + 8 * i>(w1k);
        mfma32_p<i == 0>(X, ax[i], bfr);
        if constexpr (i < 3) dma_go(wsd, vO);
      });
      MFMA_DONE2(X, Y);
      const float rd = __builtin_amdgcn_rcpf(Y[4] + 1e-6f);
#pragma unroll
      for (int i = 0; i < 8; ++i) oo[i] = (v2f){X[2 * i], X[2 * i + 1]} * (v2f){rd, rd};
      oo[8] = (v2f){Y[0], Y[1]} * (v2f){rd, rd}; oo[9] = (v2f){Y[2], Y[3]} * (v2f){rd, rd};
    }
    SB();
    {
      const u32 ob = o_pub + (u32)L * (128u * 20480u);
      st16((v4u){pk2(oo[0].x, oo[0].y), pk2(oo[1].x, oo[1].y), pk2(oo[2].x, oo[2].y), pk2(oo[3].x, oo[3].y)}, ws, oA, ob);
      st16((v4u){pk2(oo[4].x, oo[4].y), pk2(oo[5].x, oo[5].y), pk2(oo[6].x, oo[6].y), pk2(oo[7].x, oo[7].y)}, ws, oA, ob + 256u);
      st8((v2u){pk2(oo[8].x, oo[8].y), pk2(oo[9].x, oo[9].y)}, ws, oT8, ob);
    }
    SB();
    STAMP(4 + L * 14 + 8);
    if (L == 0) {
      flag_check(flag_get<19 + NPE>(), pkF(PH_F1, true), 31);
      flag_put(wsd, pkF(PH_F2, false));
      sfor<0, NPE>([&](auto ic) { park_load<ic.value>(wsd, lw16, imgN); });
    }
    sfor<NPE, NPK>([&](auto ic) { park_load<ic.value>(wsd, lw16, imgN); });
    vmwait<3 + NPK - NPE>();
    asm volatile("s_barrier" ::: "memory");
    const lptr w0 = LP(addc<L_WO>(l16)), w1 = LP(addc<L_WO + 10 * 5120>(l16));
    const u32 vN = adds(l16, imgN);
    v2f res[10];
#pragma unroll
    for (int c = 0; c < 5; ++c) { res[2 * c] = (v2f){bflo(xres[c].x), bfhi(xres[c].x)}; res[2 * c + 1] = (v2f){bflo(xres[c].y), bfhi(xres[c].y)}; }
    asm volatile("" : "+v"(res[0]), "+v"(res[1]), "+v"(res[2]), "+v"(res[3]), "+v"(res[4]), "+v"(res[5]), "+v"(res[6]), "+v"(res[7]), "+v"(res[8]), "+v"(res[9]));
    const u32 ob = WS_O + (u32)((L * 16 + tg) * 8 + th * 4 + w) * 20480u;
    v4u fa[3][5];
    auto pp_begin = [&]() {
#pragma unroll
      for (int c = 0; c < 5; ++c) fa[0][c] = *(LAS v4u*)(w0 + c * 1024);
#pragma unroll
      for (int c = 0; c < 5; ++c) fa[1][c] = *(LAS v4u*)(w0 + 5120 + c * 1024);
      SB();
    };
    v4f ac[5];
    auto pp_run = [&]() {
      sfor<0, 20>([&](auto ic) {
        constexpr int s = ic.value, n = s + 2;
        const lptr wb = (n < 10 ? w0 : w1) + (n % 10) * 5120;
        sfor<0, 5>([&](auto jc) {
          constexpr int c = jc.value;
          constexpr int q = (s >= NO && ((c == 0 && pp_cnt(s) >= 1) || (c == 3 && pp_cnt(s) == 2))) ? pp_slot(s) + (c == 3) : -1;
          if constexpr (q >= 19 - NPK) dma_set<4 * (q - (19 - NPK))>(w1k);
          if constexpr (c == 0) mfma16_xw<s, s == 0, pp_wait(s)>(ac[0], fa[s % 3]);
          else mfma16_x<s, s == 0>(ac[c], asbf(fa[s % 3][c]));
          if constexpr (c == 0 && s < NO) ld_frag<WIN + s>(wsd, l16, ob);
          if constexpr (q >= 0 && q < 19 - NPK) park_load<NPK + q>(wsd, lw16, imgN);
          if constexpr (q >= 19 - NPK) dma_go(wsd, vN);
          SB();
          if constexpr (n < 20) fa[n % 3][c] = *(LAS v4u*)(wb + c * 1024);
          SB();
        });
      });
      asm volatile("s_nop 7" : "+v"(ac[0]), "+v"(ac[1]), "+v"(ac[2]), "+v"(ac[3]), "+v"(ac[4]));
    };
    pp_begin();
    if (L == 0) flag_check(flag_get<NPK>(), pkF(PH_F2, false), 31);
    else if (ty == 0) __builtin_amdgcn_s_sleep(SLP_O); else __builtin_amdgcn_s_sleep(SLP_OB);
    STAMP(4 + L * 14 + 9);
    flag_put(wsd, poO, ob);
    frag_window(wsd, l16, ob);
    if (__builtin_expect(__any(flag_get<WIN>() == SENT), 0)) {
      COUNT(12 + L);
      for (u32 sp = 0; sp < SPIN_CAP; ++sp) {
        flag_put(wsd, poO, ob);
        frag_window(wsd, l16, ob);
        if (!__any(flag_get<WIN>() == SENT)) break;
      }
    }
    STAMP(4 + L * 14 + 10);
    pp_run();
    if (__builtin_expect(__any(ac[0][0] != ac[0][0]) || (FORCE == 3 && w == (j & 3)), 0)) {
      COUNT(4 + L);
      strict_wait(ws, fresh(l16), ob, 20);
      frag_window(wsd, l16, ob);
      pp_begin();
      pp_run();
    }
    STAMP(4 + L * 14 + 11);
#pragma unroll
    for (int c = 0; c < 5; ++c) { res[2 * c] += ac[c].xy; res[2 * c + 1] += ac[c].zw; }
    SB();
    v2u xr[5];
#pragma unroll
    for (int c = 0; c < 5; ++c) { xr[c].x = pk2(res[2 * c].x, res[2 * c].y); xr[c].y = pk2(res[2 * c + 1].x, res[2 * c + 1].y); }
    if (L == 3) {
      const int ln = fresh(lane), gg = ln >> 4;
      GAS char* op = (GAS char*)p.out + ((128 * tg + 64 * th + 16 * w + (ln & 15)) * 1280 + 160 * cg);
#pragma unroll
      for (int c = 0; c < 4; c += 2) {
        auto r0 = __builtin_amdgcn_permlane16_swap(xr[c].x, xr[c + 1].x, false, false);
        auto r1 = __builtin_amdgcn_permlane16_swap(xr[c].y, xr[c + 1].y, false, false);
        *(GAS v4u*)(op + 32 * c + (gg & 1) * 32 + (gg >> 1) * 16) = (v4u){r0[0], r1[0], r0[1], r1[1]};
      }
      *(GAS v2u*)(op + 128 + 8 * gg) = xr[4];
      if (b == 0) st4(E + 16u, ws, one0, 0);
      STAMP(59);
      break;
    }
    {
#pragma unroll
      for (int c = 0; c < 4; c += 2) {
        auto r0 = __builtin_amdgcn_permlane16_swap(xr[c].x, xr[c + 1].x, false, false);
        auto r1 = __builtin_amdgcn_permlane16_swap(xr[c].y, xr[c + 1].y, false, false);
        st16((v4u){r0[0], r1[0], r0[1], r1[1]}, ws, xA, xn + (u32)c * 1024u);
      }
      st8(xr[4], ws, xT8, xn);
      SB();
      asm volatile("s_barrier" ::: "memory");
      STAMP(4 + L * 14 + 12);
      const lptr pk0 = LP(addc<36864>(lw16)), pk1 = LP(addc<36864 + 40960>(lw16));
      auto unpark = [&](auto lo, auto hi, auto extra) {
        sfor<decltype(lo)::value, decltype(hi)::value>([&](auto ic) {
          if constexpr (ic.value == NPK) vmwait<12 + decltype(extra)::value>();
          park_store<ic.value>(ic.value < 10 ? pk0 : pk1);
        });
      };
      if (L == 0) {
        v4u cq4[4];
        cs_load(cq4, 1);
        unpark(IC<0>{}, IC<NUP>{}, IC<4>{});
        wait4<0>(cq4);
        *(LAS v2f*)(LP(addc<L_CSP>(fresh(lane) * 8)) + w * 512) = cs_check(cq4, 1);
      } else {
        unpark(IC<0>{}, IC<NUP>{}, IC<0>{});
      }
#pragma unroll
      for (int c = 0; c < 5; ++c) xres[c] = xr[c];
      st16(row_sums(xres, tagL + 1u), ws, stS, WS_ST + (u32)((((L + 1) * 16 + tg) * 4 + th * 2 + (w >> 1)) * 8 + cg) * 512u);
      SB();
      __builtin_amdgcn_s_sleep(SLP_X);
      STAMP(4 + L * 14 + 6);
      flag_put(wsd, poX, xb + 64u * 40960u);
      frag_window(wsd, l16, xb + 64u * 40960u);
      unpark(IC<NUP>{}, IC<19>{}, IC<WIN + 2>{});
      vmwait<5 + WIN>();
      wg_barrier();
      if (L == 0) cs_final(1);
    }
    STAMP(4 + L * 14 + 13);
    ++L;
  }
  DRAIN();
}

extern "C" void kernel_launch(void* const* d_in, const int* in_sizes, int n_in, void* d_out, int out_size, void* d_ws,
                              size_t ws_size, hipStream_t stream) {
  Params p;
  p.x0 = (const u16*)d_in[0]; p.wqA = (const u16*)d_in[1]; p.woA = (const u16*)d_in[2];
  p.wqB = (const u16*)d_in[4]; p.wphi = (const u16*)d_in[5]; p.woB = (const u16*)d_in[6];
  p.out = (u16*)d_out; p.ws = (unsigned char*)d_ws;
  (void)hipFuncSetAttribute((const void*)fused_kernel, hipFuncAttributeMaxDynamicSharedMemorySize, LDS_BYTES);
  fused_kernel<<<256, 256, LDS_BYTES, stream>>>(p);
}
```
